```python
import torch

def _softmax(x, axis=-1):
    return torch.softmax(x, dim=axis)
def _gelu(x):
    return torch.nn.functional.gelu(x, approximate="tanh")
def _silu(x):
    return torch.nn.functional.silu(x)
def _sigmoid(x):
    return torch.sigmoid(x)
def _layernorm(x, axis=-1, eps=1e-5):
    m = x.mean(dim=axis, keepdim=True)
    v = x.var(dim=axis, keepdim=True, unbiased=False)
    return (x - m) / torch.sqrt(v + eps)
def _scatter_add(tgt, idx, src, axis=0):
    return tgt.clone().index_add_(axis, idx, src)
def _segment_sum(src, seg, n):
    out = torch.zeros((n,) + src.shape[1:], dtype=src.dtype)
    return out.index_add_(0, seg, src)
def _pad(x, pw):
    flat = tuple(v for pair in reversed(pw) for v in pair)
    return torch.nn.functional.pad(x, flat)

kernel_name = 'parallel_sum__grouped_kv_mqa__dense_activation_squared_relu__prompt'

def setup_inputs():

    _w = (lambda a: (lambda t: t.to(torch.bfloat16) if t.is_floating_point() else t)(torch.as_tensor(a)))
    def _A_setup():
        B, S, D, H = 1, 2048, 640, 32
        Dh = D // H
        return {
            "x": _w((lambda *s: torch.randn(*s, dtype=torch.bfloat16) * 0.05)(B, S, D)),
            "wq": _w((lambda *s: torch.randn(*s, dtype=torch.bfloat16) * 0.2)(D, H * Dh)),
            "wk": _w((lambda *s: torch.randn(*s, dtype=torch.bfloat16) * 0.2)(D, Dh)),
            "wv": _w((lambda *s: torch.randn(*s, dtype=torch.bfloat16) * 0.1)(D, Dh)),
            "wo": _w((lambda *s: torch.randn(*s, dtype=torch.bfloat16) * 0.1)(H * Dh, D)),
        }
    _A_in = _A_setup()

    def _B_setup():
        B, S, D, F = 1, 2048, 640, 1536
        return {
            "x": _w((lambda *s: torch.randn(*s, dtype=torch.bfloat16) * 0.05)(B, S, D)),
            "w1": _w((lambda *s: torch.randn(*s, dtype=torch.bfloat16) * 0.1)(D, F)),
            "b1": _w((lambda *s: torch.randn(*s, dtype=torch.bfloat16) * 0.4)(F)),
            "w2": _w((lambda *s: torch.randn(*s, dtype=torch.bfloat16) * 0.1)(F, D)),
            "b2": _w((lambda *s: torch.randn(*s, dtype=torch.bfloat16) * 12.8)(D)),
        }
    _B_in = _B_setup()

    return {
        "A_x": _A_in["x"],
        "A_wq": _A_in["wq"],
        "A_wk": _A_in["wk"],
        "A_wv": _A_in["wv"],
        "A_wo": _A_in["wo"],
        "B_x": _B_in["x"],
        "B_w1": _B_in["w1"],
        "B_b1": _B_in["b1"],
        "B_w2": _B_in["w2"],
        "B_b2": _B_in["b2"],
    }

def reference(A_x, A_wq, A_wk, A_wv, A_wo, B_x, B_w1, B_b1, B_w2, B_b2):
    def _A_ref(x, wq, wk, wv, wo):
        B, S, D = x.shape
        H = 32
        Dh = D // H
        q = (lambda x, *d: x.permute(*d))((x @ wq).reshape(B, S, H, Dh), 0, 2, 1, 3)
        k = x @ wk
        v = x @ wv
        scores = torch.einsum("bhqd,bkd->bhqk", q, k) * (Dh ** -0.5)
        p = _softmax(scores, axis=-1)
        o = torch.einsum("bhqk,bkd->bhqd", p, v)
        return (lambda x, *d: x.permute(*d))(o, 0, 2, 1, 3).reshape(B, S, H * Dh) @ wo

    def _B_ref(x, w1, b1, w2, b2):
        h = torch.einsum("bsd,df->bsf", x, w1) + b1
        zero = torch.zeros_like(h)
        r = torch.where(h > zero, h, zero)
        h = r * r
        out = torch.einsum("bsf,fd->bsd", h, w2) + b2
        return out

    x = A_x
    xn = _layernorm(x, axis=-1)
    return x + _A_ref(xn, A_wq, A_wk, A_wv, A_wo) + _B_ref(xn, B_w1, B_b1, B_w2, B_b2)
```

```cpp
#include <hip/hip_runtime.h>
#include <hip/hip_bf16.h>
typedef __hip_bfloat16 bf16;
constexpr int S=2048, D=640, H=32, DH=20, F=1536;
__device__ __forceinline__ float b2f(bf16 x){ return __bfloat162float(x); }
__global__ void k_ln(const bf16* x, bf16* xn){
  int row=blockIdx.x; __shared__ float red[2][4];
  float v[3]; float s=0;
  for(int i=0;i<3;i++){ int c=threadIdx.x+i*256; v[i]= c<D? b2f(x[row*D+c]):0.f; s+=v[i]; }
  for(int o=32;o;o>>=1) s+=__shfl_xor(s,o);
  if((threadIdx.x&63)==0) red[0][threadIdx.x>>6]=s; __syncthreads();
  float m=(red[0][0]+red[0][1]+red[0][2]+red[0][3])/D;
  float q=0; for(int i=0;i<3;i++){ int c=threadIdx.x+i*256; float d= c<D? v[i]-m:0.f; q+=d*d; }
  for(int o=32;o;o>>=1) q+=__shfl_xor(q,o);
  if((threadIdx.x&63)==0) red[1][threadIdx.x>>6]=q; __syncthreads();
  float var=(red[1][0]+red[1][1]+red[1][2]+red[1][3])/D; float r=1.f/sqrtf(var+1e-5f);
  for(int i=0;i<3;i++){ int c=threadIdx.x+i*256; if(c<D) xn[row*D+c]=__float2bfloat16((v[i]-m)*r); }
}
template<int EPI>
__global__ void k_gemm(const bf16* A,const bf16* B,const bf16* bias,bf16* Cb,float* Cf,int M,int N,int K,int ldc){
  __shared__ float As[16][33], Bs[16][33];
  int tx=threadIdx.x&15, ty=threadIdx.x>>4; int m0=blockIdx.y*32, n0=blockIdx.x*32;
  float acc[2][2]={};
  for(int k0=0;k0<K;k0+=16){
    for(int i=threadIdx.x;i<512;i+=256){ int r=i>>4,c=i&15; As[c][r]=b2f(A[(m0+r)*K+k0+c]); }
    for(int i=threadIdx.x;i<512;i+=256){ int r=i>>5,c=i&31; Bs[r][c]=(n0+c<N)? b2f(B[(k0+r)*N+n0+c]):0.f; }
    __syncthreads();
    for(int k=0;k<16;k++){ float a0=As[k][ty*2],a1=As[k][ty*2+1],b0=Bs[k][tx*2],b1=Bs[k][tx*2+1];
      acc[0][0]+=a0*b0;acc[0][1]+=a0*b1;acc[1][0]+=a1*b0;acc[1][1]+=a1*b1; }
    __syncthreads();
  }
  for(int i=0;i<2;i++)for(int j=0;j<2;j++){ int m=m0+ty*2+i,n=n0+tx*2+j; if(n<N){ float v=acc[i][j];
    if(EPI==1){ v+=b2f(bias[n]); v=v>0?v:0; v=v*v; }
    if(EPI==2) Cf[m*ldc+n]=v; else if(EPI==3) Cf[m*ldc+n]+=v; else Cb[m*ldc+n]=__float2bfloat16(v); } }
}
__global__ void k_attn(const bf16* q,const bf16* k,const bf16* v,bf16* o){
  __shared__ float Ks[64][DH], Vs[64][DH];
  int idx=blockIdx.x*256+threadIdx.x; int qi=idx>>5, h=idx&31;
  float qv[DH]; for(int d=0;d<DH;d++) qv[d]=b2f(q[qi*D+h*DH+d])*0.22360679775f;
  float m=-1e30f,l=0,acc[DH]={};
  for(int k0=0;k0<S;k0+=64){
    __syncthreads();
    for(int i=threadIdx.x;i<64*DH;i+=256){ Ks[i/DH][i%DH]=b2f(k[(k0)*DH+i]); Vs[i/DH][i%DH]=b2f(v[(k0)*DH+i]); }
    __syncthreads();
    for(int j=0;j<64;j++){ float s=0; for(int d=0;d<DH;d++) s+=qv[d]*Ks[j][d];
      float mn=fmaxf(m,s); float a=__expf(m-mn), p=__expf(s-mn); l=l*a+p; for(int d=0;d<DH;d++) acc[d]=acc[d]*a+p*Vs[j][d]; m=mn; }
  }
  for(int d=0;d<DH;d++) o[qi*D+h*DH+d]=__float2bfloat16(acc[d]/l);
}
__global__ void k_fin(const bf16* x,const float* acc,const bf16* b2,bf16* out){
  int i=blockIdx.x*256+threadIdx.x; out[i]=__float2bfloat16(b2f(x[i])+acc[i]+b2f(b2[i%D]));
}
extern "C" void kernel_launch(void* const* d_in, const int* in_sizes, int n_in, void* d_out, int out_size, void* d_ws, size_t ws_size, hipStream_t stream){
  const bf16* x=(const bf16*)d_in[0]; const bf16* wq=(const bf16*)d_in[1]; const bf16* wk=(const bf16*)d_in[2]; const bf16* wv=(const bf16*)d_in[3]; const bf16* wo=(const bf16*)d_in[4];
  const bf16* w1=(const bf16*)d_in[6]; const bf16* b1=(const bf16*)d_in[7]; const bf16* w2=(const bf16*)d_in[8]; const bf16* b2=(const bf16*)d_in[9];
  char* ws=(char*)d_ws; bf16* xn=(bf16*)ws; bf16* q=(bf16*)(ws+(4<<20)); bf16* k=(bf16*)(ws+(8<<20)); bf16* v=(bf16*)(ws+(9<<20)); bf16* h=(bf16*)(ws+(10<<20)); bf16* o=(bf16*)(ws+(20<<20)); float* acc=(float*)(ws+(24<<20));
  k_ln<<<S,256,0,stream>>>(x,xn);
  k_gemm<0><<<dim3(D/32,S/32),256,0,stream>>>(xn,wq,nullptr,q,nullptr,S,D,D,D);
  k_gemm<0><<<dim3(1,S/32),256,0,stream>>>(xn,wk,nullptr,k,nullptr,S,DH,D,DH);
  k_gemm<0><<<dim3(1,S/32),256,0,stream>>>(xn,wv,nullptr,v,nullptr,S,DH,D,DH);
  k_gemm<1><<<dim3(F/32,S/32),256,0,stream>>>(xn,w1,b1,h,nullptr,S,F,D,F);
  k_attn<<<S*H/256,256,0,stream>>>(q,k,v,o);
  k_gemm<2><<<dim3(D/32,S/32),256,0,stream>>>(o,wo,nullptr,nullptr,acc,S,D,D,D);
  k_gemm<3><<<dim3(D/32,S/32),256,0,stream>>>(h,w2,nullptr,nullptr,acc,S,D,F,D);
  k_fin<<<S*D/256,256,0,stream>>>(x,acc,b2,(bf16*)d_out);
}
```

```cpp
#include <hip/hip_runtime.h>
#include <hip/hip_bf16.h>

extern "C" __global__ __launch_bounds__(512) void fused_block_kernel(const void* x, const void* wq, const void* wk, const void* wv, const void* wo,
                                                                    const void* w1, const void* b1, const void* w2, const void* b2, void* out, void* ws) {
  __shared__ __attribute__((aligned(16))) char lds[163840];
  asm volatile("" :: "v"(lds));
  asm volatile(
"s_mov_b64 s[4:5], %0\n"
"s_mov_b32 s6, %1\n"
"v_and_b32 v1, 63, %2\n"
"s_load_dwordx16 s[8:23], s[4:5], 0x0\n"
"s_load_dwordx4 s[24:27], s[4:5], 0x40\n"
"s_load_dwordx2 s[28:29], s[4:5], 0x50\n"
"v_lshrrev_b32 v4, 6, %2\n"
"s_nop 0\n"
"v_readfirstlane_b32 s7, v4\n"
"v_and_b32 v2, 31, v1\n"
"v_lshrrev_b32 v3, 5, v1\n"
"s_and_b32 s60, s6, 7\n"
"s_lshr_b32 s61, s6, 3\n"
"s_lshr_b32 s62, s60, 1\n"
"s_lshl_b32 s62, s62, 3\n"
"s_lshr_b32 s63, s61, 2\n"
"s_add_u32 s30, s62, s63\n"
"s_and_b32 s62, s60, 1\n"
"s_lshl_b32 s62, s62, 2\n"
"s_and_b32 s63, s61, 3\n"
"s_add_u32 s31, s62, s63\n"
"s_waitcnt lgkmcnt(0)\n"
"s_load_dwordx2 s[32:33], s[28:29], 0x0\n"
"s_mov_b32 s40, s8\n"
"s_and_b32 s41, s9, 0xffff\n"
"s_mov_b32 s42, 2621440\n"
"s_mov_b32 s43, 0x20000\n"
"s_mov_b32 s36, s28\n"
"s_and_b32 s37, s29, 0xffff\n"
"s_mov_b32 s38, 33554432\n"
"s_mov_b32 s39, 0x20000\n"
"v_lshrrev_b32 v4, 3, %2\n"
"v_and_b32 v5, 7, %2\n"
"s_lshr_b32 s60, s31, 2\n"
"s_lshl_b32 s60, s60, 5\n"
"v_xor_b32 v4, s60, v4\n"
"s_lshl_b32 s60, s30, 6\n"
"v_add_u32 v6, s60, v4\n"
"v_mul_u32_u24 v6, 1280, v6\n"
"v_lshl_add_u32 v6, v5, 4, v6\n"
"buffer_load_dwordx4 v[8:11], v6, s[40:43], 0 offen offset:0\n"
"buffer_load_dwordx4 v[12:15], v6, s[40:43], 0 offen offset:128\n"
"buffer_load_dwordx4 v[16:19], v6, s[40:43], 0 offen offset:256\n"
"buffer_load_dwordx4 v[20:23], v6, s[40:43], 0 offen offset:384\n"
"buffer_load_dwordx4 v[24:27], v6, s[40:43], 0 offen offset:512\n"
"buffer_load_dwordx4 v[28:31], v6, s[40:43], 0 offen offset:640\n"
"buffer_load_dwordx4 v[32:35], v6, s[40:43], 0 offen offset:768\n"
"buffer_load_dwordx4 v[36:39], v6, s[40:43], 0 offen offset:896\n"
"buffer_load_dwordx4 v[40:43], v6, s[40:43], 0 offen offset:1024\n"
"buffer_load_dwordx4 v[44:47], v6, s[40:43], 0 offen offset:1152\n"
"v_mul_u32_u24 v7, 1296, v4\n"
"v_lshl_add_u32 v7, v5, 4, v7\n"
"v_add_u32 v7, 78848, v7\n"
"s_waitcnt lgkmcnt(0)\n"
"s_add_u32 s32, s32, 1\n"
"s_addc_u32 s33, s33, 0\n"
"s_cmp_ge_u32 s7, 4\n"
"s_cbranch_scc1 L_aux\n"
"s_and_b32 s61, s7, 1\n"
"s_lshl_b32 s34, s61, 3\n"
"s_bitcmp1_b32 s31, 1\n"
"s_cselect_b32 s60, s14, s12\n"
"s_cselect_b32 s61, s15, s13\n"
"s_mov_b32 s52, s60\n"
"s_and_b32 s53, s61, 0xffff\n"
"s_bitcmp1_b32 s31, 0\n"
"s_cselect_b32 s54, 0, 25600\n"
"s_mov_b32 s55, 0x20000\n"
"s_mul_i32 s60, s7, 6400\n"
"v_lshlrev_b32 v48, 4, v1\n"
"v_add_u32 v48, s60, v48\n"
"s_add_u32 s35, s60, 53248\n"
"s_cmp_lg_u32 s7, 0\n"
"s_cbranch_scc1 L_nozero\n"
"v_lshlrev_b32 v244, 2, v1\n"
"v_add_u32 v244, 161792, v244\n"
"v_mov_b32 v245, 0\n"
"ds_write_b32 v244, v245\n"
"s_waitcnt lgkmcnt(0)\n"
"L_nozero:\n"
"s_barrier\n"
"s_add_u32 s61, s35, 0\n"
"s_mov_b32 m0, s61\n"
"s_mov_b32 s62, 0\n"
"buffer_load_dwordx4 v48, s[52:55], s62 offen lds\n"
"s_add_u32 s61, s35, 1024\n"
"s_mov_b32 m0, s61\n"
"s_mov_b32 s62, 1024\n"
"buffer_load_dwordx4 v48, s[52:55], s62 offen lds\n"
"s_add_u32 s61, s35, 2048\n"
"s_mov_b32 m0, s61\n"
"s_mov_b32 s62, 2048\n"
"buffer_load_dwordx4 v48, s[52:55], s62 offen lds\n"
"s_add_u32 s61, s35, 3072\n"
"s_mov_b32 m0, s61\n"
"s_mov_b32 s62, 3072\n"
"buffer_load_dwordx4 v48, s[52:55], s62 offen lds\n"
"s_add_u32 s61, s35, 4096\n"
"s_mov_b32 m0, s61\n"
"s_mov_b32 s62, 4096\n"
"buffer_load_dwordx4 v48, s[52:55], s62 offen lds\n"
"s_add_u32 s61, s35, 5120\n"
"s_mov_b32 m0, s61\n"
"s_mov_b32 s62, 5120\n"
"buffer_load_dwordx4 v48, s[52:55], s62 offen lds\n"
"s_add_u32 s61, s35, 6144\n"
"s_mov_b32 m0, s61\n"
"s_mov_b32 s62, 6144\n"
"s_mov_b64 exec, 0xffff\n"
"buffer_load_dwordx4 v48, s[52:55], s62 offen lds\n"
"s_mov_b64 exec, -1\n"
"s_mov_b32 s60, 0x3f803f80\n"
"v_mov_b32 v240, 0\n"
"v_mov_b32 v241, 0\n"
"v_mov_b32 v242, 0\n"
"v_mov_b32 v243, 0\n"
"s_waitcnt vmcnt(16)\n"
"v_dot2_f32_bf16 v240, v8, s60, v240\n"
"v_dot2_f32_bf16 v242, v8, v8, v242\n"
"v_dot2_f32_bf16 v241, v9, s60, v241\n"
"v_dot2_f32_bf16 v243, v9, v9, v243\n"
"v_dot2_f32_bf16 v240, v10, s60, v240\n"
"v_dot2_f32_bf16 v242, v10, v10, v242\n"
"v_dot2_f32_bf16 v241, v11, s60, v241\n"
"v_dot2_f32_bf16 v243, v11, v11, v243\n"
"s_waitcnt vmcnt(15)\n"
"v_dot2_f32_bf16 v240, v12, s60, v240\n"
"v_dot2_f32_bf16 v242, v12, v12, v242\n"
"v_dot2_f32_bf16 v241, v13, s60, v241\n"
"v_dot2_f32_bf16 v243, v13, v13, v243\n"
"v_dot2_f32_bf16 v240, v14, s60, v240\n"
"v_dot2_f32_bf16 v242, v14, v14, v242\n"
"v_dot2_f32_bf16 v241, v15, s60, v241\n"
"v_dot2_f32_bf16 v243, v15, v15, v243\n"
"s_waitcnt vmcnt(14)\n"
"v_dot2_f32_bf16 v240, v16, s60, v240\n"
"v_dot2_f32_bf16 v242, v16, v16, v242\n"
"v_dot2_f32_bf16 v241, v17, s60, v241\n"
"v_dot2_f32_bf16 v243, v17, v17, v243\n"
"v_dot2_f32_bf16 v240, v18, s60, v240\n"
"v_dot2_f32_bf16 v242, v18, v18, v242\n"
"v_dot2_f32_bf16 v241, v19, s60, v241\n"
"v_dot2_f32_bf16 v243, v19, v19, v243\n"
"s_waitcnt vmcnt(13)\n"
"v_dot2_f32_bf16 v240, v20, s60, v240\n"
"v_dot2_f32_bf16 v242, v20, v20, v242\n"
"v_dot2_f32_bf16 v241, v21, s60, v241\n"
"v_dot2_f32_bf16 v243, v21, v21, v243\n"
"v_dot2_f32_bf16 v240, v22, s60, v240\n"
"v_dot2_f32_bf16 v242, v22, v22, v242\n"
"v_dot2_f32_bf16 v241, v23, s60, v241\n"
"v_dot2_f32_bf16 v243, v23, v23, v243\n"
"s_waitcnt vmcnt(12)\n"
"v_dot2_f32_bf16 v240, v24, s60, v240\n"
"v_dot2_f32_bf16 v242, v24, v24, v242\n"
"v_dot2_f32_bf16 v241, v25, s60, v241\n"
"v_dot2_f32_bf16 v243, v25, v25, v243\n"
"v_dot2_f32_bf16 v240, v26, s60, v240\n"
"v_dot2_f32_bf16 v242, v26, v26, v242\n"
"v_dot2_f32_bf16 v241, v27, s60, v241\n"
"v_dot2_f32_bf16 v243, v27, v27, v243\n"
"s_waitcnt vmcnt(11)\n"
"v_dot2_f32_bf16 v240, v28, s60, v240\n"
"v_dot2_f32_bf16 v242, v28, v28, v242\n"
"v_dot2_f32_bf16 v241, v29, s60, v241\n"
"v_dot2_f32_bf16 v243, v29, v29, v243\n"
"v_dot2_f32_bf16 v240, v30, s60, v240\n"
"v_dot2_f32_bf16 v242, v30, v30, v242\n"
"v_dot2_f32_bf16 v241, v31, s60, v241\n"
"v_dot2_f32_bf16 v243, v31, v31, v243\n"
"s_waitcnt vmcnt(10)\n"
"v_dot2_f32_bf16 v240, v32, s60, v240\n"
"v_dot2_f32_bf16 v242, v32, v32, v242\n"
"v_dot2_f32_bf16 v241, v33, s60, v241\n"
"v_dot2_f32_bf16 v243, v33, v33, v243\n"
"v_dot2_f32_bf16 v240, v34, s60, v240\n"
"v_dot2_f32_bf16 v242, v34, v34, v242\n"
"v_dot2_f32_bf16 v241, v35, s60, v241\n"
"v_dot2_f32_bf16 v243, v35, v35, v243\n"
"s_waitcnt vmcnt(9)\n"
"v_dot2_f32_bf16 v240, v36, s60, v240\n"
"v_dot2_f32_bf16 v242, v36, v36, v242\n"
"v_dot2_f32_bf16 v241, v37, s60, v241\n"
"v_dot2_f32_bf16 v243, v37, v37, v243\n"
"v_dot2_f32_bf16 v240, v38, s60, v240\n"
"v_dot2_f32_bf16 v242, v38, v38, v242\n"
"v_dot2_f32_bf16 v241, v39, s60, v241\n"
"v_dot2_f32_bf16 v243, v39, v39, v243\n"
"s_waitcnt vmcnt(8)\n"
"v_dot2_f32_bf16 v240, v40, s60, v240\n"
"v_dot2_f32_bf16 v242, v40, v40, v242\n"
"v_dot2_f32_bf16 v241, v41, s60, v241\n"
"v_dot2_f32_bf16 v243, v41, v41, v243\n"
"v_dot2_f32_bf16 v240, v42, s60, v240\n"
"v_dot2_f32_bf16 v242, v42, v42, v242\n"
"v_dot2_f32_bf16 v241, v43, s60, v241\n"
"v_dot2_f32_bf16 v243, v43, v43, v243\n"
"s_waitcnt vmcnt(7)\n"
"v_dot2_f32_bf16 v240, v44, s60, v240\n"
"v_dot2_f32_bf16 v242, v44, v44, v242\n"
"v_dot2_f32_bf16 v241, v45, s60, v241\n"
"v_dot2_f32_bf16 v243, v45, v45, v243\n"
"v_dot2_f32_bf16 v240, v46, s60, v240\n"
"v_dot2_f32_bf16 v242, v46, v46, v242\n"
"v_dot2_f32_bf16 v241, v47, s60, v241\n"
"v_dot2_f32_bf16 v243, v47, v47, v243\n"
"s_nop 1\n"
"v_add_f32 v240, v240, v241\n"
"v_add_f32 v242, v242, v243\n"
"s_nop 0\n"
"v_add_f32_dpp v240, v240, v240 quad_perm:[1,0,3,2] row_mask:0xf bank_mask:0xf\n"
"v_add_f32_dpp v242, v242, v242 quad_perm:[1,0,3,2] row_mask:0xf bank_mask:0xf\n"
"s_nop 0\n"
"v_add_f32_dpp v240, v240, v240 quad_perm:[2,3,0,1] row_mask:0xf bank_mask:0xf\n"
"v_add_f32_dpp v242, v242, v242 quad_perm:[2,3,0,1] row_mask:0xf bank_mask:0xf\n"
"s_nop 0\n"
"v_add_f32_dpp v240, v240, v240 row_half_mirror row_mask:0xf bank_mask:0xf\n"
"v_add_f32_dpp v242, v242, v242 row_half_mirror row_mask:0xf bank_mask:0xf\n"
"v_mul_f32 v241, 0x3acccccd, v240\n"
"v_mul_f32 v243, 0x3acccccd, v242\n"
"v_fma_f32 v243, -v241, v241, v243\n"
"v_max_f32 v243, 0, v243\n"
"v_add_f32 v243, 0x3727c5ac, v243\n"
"v_rsq_f32 v244, v243\n"
"s_nop 0\n"
"v_mul_f32 v246, v241, v244\n"
"v_sub_f32 v246, 0, v246\n"
"v_lshlrev_b32 v248, 16, v8\n"
"v_and_b32 v249, 0xffff0000, v8\n"
"v_pk_fma_f32 v[248:249], v[248:249], v[244:245], v[246:247] op_sel_hi:[1,0,0]\n"
"v_cvt_pk_bf16_f32 v8, v248, v249\n"
"v_lshlrev_b32 v250, 16, v9\n"
"v_and_b32 v251, 0xffff0000, v9\n"
"v_pk_fma_f32 v[250:251], v[250:251], v[244:245], v[246:247] op_sel_hi:[1,0,0]\n"
"v_cvt_pk_bf16_f32 v9, v250, v251\n"
"v_lshlrev_b32 v248, 16, v10\n"
"v_and_b32 v249, 0xffff0000, v10\n"
"v_pk_fma_f32 v[248:249], v[248:249], v[244:245], v[246:247] op_sel_hi:[1,0,0]\n"
"v_cvt_pk_bf16_f32 v10, v248, v249\n"
"v_lshlrev_b32 v250, 16, v11\n"
"v_and_b32 v251, 0xffff0000, v11\n"
"v_pk_fma_f32 v[250:251], v[250:251], v[244:245], v[246:247] op_sel_hi:[1,0,0]\n"
"v_cvt_pk_bf16_f32 v11, v250, v251\n"
"ds_write_b128 v7, v[8:11] offset:0\n"
"v_lshlrev_b32 v248, 16, v12\n"
"v_and_b32 v249, 0xffff0000, v12\n"
"v_pk_fma_f32 v[248:249], v[248:249], v[244:245], v[246:247] op_sel_hi:[1,0,0]\n"
"v_cvt_pk_bf16_f32 v12, v248, v249\n"
"v_lshlrev_b32 v250, 16, v13\n"
"v_and_b32 v251, 0xffff0000, v13\n"
"v_pk_fma_f32 v[250:251], v[250:251], v[244:245], v[246:247] op_sel_hi:[1,0,0]\n"
"v_cvt_pk_bf16_f32 v13, v250, v251\n"
"v_lshlrev_b32 v248, 16, v14\n"
"v_and_b32 v249, 0xffff0000, v14\n"
"v_pk_fma_f32 v[248:249], v[248:249], v[244:245], v[246:247] op_sel_hi:[1,0,0]\n"
"v_cvt_pk_bf16_f32 v14, v248, v249\n"
"v_lshlrev_b32 v250, 16, v15\n"
"v_and_b32 v251, 0xffff0000, v15\n"
"v_pk_fma_f32 v[250:251], v[250:251], v[244:245], v[246:247] op_sel_hi:[1,0,0]\n"
"v_cvt_pk_bf16_f32 v15, v250, v251\n"
"ds_write_b128 v7, v[12:15] offset:128\n"
"v_lshlrev_b32 v248, 16, v16\n"
"v_and_b32 v249, 0xffff0000, v16\n"
"v_pk_fma_f32 v[248:249], v[248:249], v[244:245], v[246:247] op_sel_hi:[1,0,0]\n"
"v_cvt_pk_bf16_f32 v16, v248, v249\n"
"v_lshlrev_b32 v250, 16, v17\n"
"v_and_b32 v251, 0xffff0000, v17\n"
"v_pk_fma_f32 v[250:251], v[250:251], v[244:245], v[246:247] op_sel_hi:[1,0,0]\n"
"v_cvt_pk_bf16_f32 v17, v250, v251\n"
"v_lshlrev_b32 v248, 16, v18\n"
"v_and_b32 v249, 0xffff0000, v18\n"
"v_pk_fma_f32 v[248:249], v[248:249], v[244:245], v[246:247] op_sel_hi:[1,0,0]\n"
"v_cvt_pk_bf16_f32 v18, v248, v249\n"
"v_lshlrev_b32 v250, 16, v19\n"
"v_and_b32 v251, 0xffff0000, v19\n"
"v_pk_fma_f32 v[250:251], v[250:251], v[244:245], v[246:247] op_sel_hi:[1,0,0]\n"
"v_cvt_pk_bf16_f32 v19, v250, v251\n"
"ds_write_b128 v7, v[16:19] offset:256\n"
"v_lshlrev_b32 v248, 16, v20\n"
"v_and_b32 v249, 0xffff0000, v20\n"
"v_pk_fma_f32 v[248:249], v[248:249], v[244:245], v[246:247] op_sel_hi:[1,0,0]\n"
"v_cvt_pk_bf16_f32 v20, v248, v249\n"
"v_lshlrev_b32 v250, 16, v21\n"
"v_and_b32 v251, 0xffff0000, v21\n"
"v_pk_fma_f32 v[250:251], v[250:251], v[244:245], v[246:247] op_sel_hi:[1,0,0]\n"
"v_cvt_pk_bf16_f32 v21, v250, v251\n"
"v_lshlrev_b32 v248, 16, v22\n"
"v_and_b32 v249, 0xffff0000, v22\n"
"v_pk_fma_f32 v[248:249], v[248:249], v[244:245], v[246:247] op_sel_hi:[1,0,0]\n"
"v_cvt_pk_bf16_f32 v22, v248, v249\n"
"v_lshlrev_b32 v250, 16, v23\n"
"v_and_b32 v251, 0xffff0000, v23\n"
"v_pk_fma_f32 v[250:251], v[250:251], v[244:245], v[246:247] op_sel_hi:[1,0,0]\n"
"v_cvt_pk_bf16_f32 v23, v250, v251\n"
"ds_write_b128 v7, v[20:23] offset:384\n"
"v_lshlrev_b32 v248, 16, v24\n"
"v_and_b32 v249, 0xffff0000, v24\n"
"v_pk_fma_f32 v[248:249], v[248:249], v[244:245], v[246:247] op_sel_hi:[1,0,0]\n"
"v_cvt_pk_bf16_f32 v24, v248, v249\n"
"v_lshlrev_b32 v250, 16, v25\n"
"v_and_b32 v251, 0xffff0000, v25\n"
"v_pk_fma_f32 v[250:251], v[250:251], v[244:245], v[246:247] op_sel_hi:[1,0,0]\n"
"v_cvt_pk_bf16_f32 v25, v250, v251\n"
"v_lshlrev_b32 v248, 16, v26\n"
"v_and_b32 v249, 0xffff0000, v26\n"
"v_pk_fma_f32 v[248:249], v[248:249], v[244:245], v[246:247] op_sel_hi:[1,0,0]\n"
"v_cvt_pk_bf16_f32 v26, v248, v249\n"
"v_lshlrev_b32 v250, 16, v27\n"
"v_and_b32 v251, 0xffff0000, v27\n"
"v_pk_fma_f32 v[250:251], v[250:251], v[244:245], v[246:247] op_sel_hi:[1,0,0]\n"
"v_cvt_pk_bf16_f32 v27, v250, v251\n"
"ds_write_b128 v7, v[24:27] offset:512\n"
"v_lshlrev_b32 v248, 16, v28\n"
"v_and_b32 v249, 0xffff0000, v28\n"
"v_pk_fma_f32 v[248:249], v[248:249], v[244:245], v[246:247] op_sel_hi:[1,0,0]\n"
"v_cvt_pk_bf16_f32 v28, v248, v249\n"
"v_lshlrev_b32 v250, 16, v29\n"
"v_and_b32 v251, 0xffff0000, v29\n"
"v_pk_fma_f32 v[250:251], v[250:251], v[244:245], v[246:247] op_sel_hi:[1,0,0]\n"
"v_cvt_pk_bf16_f32 v29, v250, v251\n"
"v_lshlrev_b32 v248, 16, v30\n"
"v_and_b32 v249, 0xffff0000, v30\n"
"v_pk_fma_f32 v[248:249], v[248:249], v[244:245], v[246:247] op_sel_hi:[1,0,0]\n"
"v_cvt_pk_bf16_f32 v30, v248, v249\n"
"v_lshlrev_b32 v250, 16, v31\n"
"v_and_b32 v251, 0xffff0000, v31\n"
"v_pk_fma_f32 v[250:251], v[250:251], v[244:245], v[246:247] op_sel_hi:[1,0,0]\n"
"v_cvt_pk_bf16_f32 v31, v250, v251\n"
"ds_write_b128 v7, v[28:31] offset:640\n"
"v_lshlrev_b32 v248, 16, v32\n"
"v_and_b32 v249, 0xffff0000, v32\n"
"v_pk_fma_f32 v[248:249], v[248:249], v[244:245], v[246:247] op_sel_hi:[1,0,0]\n"
"v_cvt_pk_bf16_f32 v32, v248, v249\n"
"v_lshlrev_b32 v250, 16, v33\n"
"v_and_b32 v251, 0xffff0000, v33\n"
"v_pk_fma_f32 v[250:251], v[250:251], v[244:245], v[246:247] op_sel_hi:[1,0,0]\n"
"v_cvt_pk_bf16_f32 v33, v250, v251\n"
"v_lshlrev_b32 v248, 16, v34\n"
"v_and_b32 v249, 0xffff0000, v34\n"
"v_pk_fma_f32 v[248:249], v[248:249], v[244:245], v[246:247] op_sel_hi:[1,0,0]\n"
"v_cvt_pk_bf16_f32 v34, v248, v249\n"
"v_lshlrev_b32 v250, 16, v35\n"
"v_and_b32 v251, 0xffff0000, v35\n"
"v_pk_fma_f32 v[250:251], v[250:251], v[244:245], v[246:247] op_sel_hi:[1,0,0]\n"
"v_cvt_pk_bf16_f32 v35, v250, v251\n"
"ds_write_b128 v7, v[32:35] offset:768\n"
"v_lshlrev_b32 v248, 16, v36\n"
"v_and_b32 v249, 0xffff0000, v36\n"
"v_pk_fma_f32 v[248:249], v[248:249], v[244:245], v[246:247] op_sel_hi:[1,0,0]\n"
"v_cvt_pk_bf16_f32 v36, v248, v249\n"
"v_lshlrev_b32 v250, 16, v37\n"
"v_and_b32 v251, 0xffff0000, v37\n"
"v_pk_fma_f32 v[250:251], v[250:251], v[244:245], v[246:247] op_sel_hi:[1,0,0]\n"
"v_cvt_pk_bf16_f32 v37, v250, v251\n"
"v_lshlrev_b32 v248, 16, v38\n"
"v_and_b32 v249, 0xffff0000, v38\n"
"v_pk_fma_f32 v[248:249], v[248:249], v[244:245], v[246:247] op_sel_hi:[1,0,0]\n"
"v_cvt_pk_bf16_f32 v38, v248, v249\n"
"v_lshlrev_b32 v250, 16, v39\n"
"v_and_b32 v251, 0xffff0000, v39\n"
"v_pk_fma_f32 v[250:251], v[250:251], v[244:245], v[246:247] op_sel_hi:[1,0,0]\n"
"v_cvt_pk_bf16_f32 v39, v250, v251\n"
"ds_write_b128 v7, v[36:39] offset:896\n"
"v_lshlrev_b32 v248, 16, v40\n"
"v_and_b32 v249, 0xffff0000, v40\n"
"v_pk_fma_f32 v[248:249], v[248:249], v[244:245], v[246:247] op_sel_hi:[1,0,0]\n"
"v_cvt_pk_bf16_f32 v40, v248, v249\n"
"v_lshlrev_b32 v250, 16, v41\n"
"v_and_b32 v251, 0xffff0000, v41\n"
"v_pk_fma_f32 v[250:251], v[250:251], v[244:245], v[246:247] op_sel_hi:[1,0,0]\n"
"v_cvt_pk_bf16_f32 v41, v250, v251\n"
"v_lshlrev_b32 v248, 16, v42\n"
"v_and_b32 v249, 0xffff0000, v42\n"
"v_pk_fma_f32 v[248:249], v[248:249], v[244:245], v[246:247] op_sel_hi:[1,0,0]\n"
"v_cvt_pk_bf16_f32 v42, v248, v249\n"
"v_lshlrev_b32 v250, 16, v43\n"
"v_and_b32 v251, 0xffff0000, v43\n"
"v_pk_fma_f32 v[250:251], v[250:251], v[244:245], v[246:247] op_sel_hi:[1,0,0]\n"
"v_cvt_pk_bf16_f32 v43, v250, v251\n"
"ds_write_b128 v7, v[40:43] offset:1024\n"
"v_lshlrev_b32 v248, 16, v44\n"
"v_and_b32 v249, 0xffff0000, v44\n"
"v_pk_fma_f32 v[248:249], v[248:249], v[244:245], v[246:247] op_sel_hi:[1,0,0]\n"
"v_cvt_pk_bf16_f32 v44, v248, v249\n"
"v_lshlrev_b32 v250, 16, v45\n"
"v_and_b32 v251, 0xffff0000, v45\n"
"v_pk_fma_f32 v[250:251], v[250:251], v[244:245], v[246:247] op_sel_hi:[1,0,0]\n"
"v_cvt_pk_bf16_f32 v45, v250, v251\n"
"v_lshlrev_b32 v248, 16, v46\n"
"v_and_b32 v249, 0xffff0000, v46\n"
"v_pk_fma_f32 v[248:249], v[248:249], v[244:245], v[246:247] op_sel_hi:[1,0,0]\n"
"v_cvt_pk_bf16_f32 v46, v248, v249\n"
"v_lshlrev_b32 v250, 16, v47\n"
"v_and_b32 v251, 0xffff0000, v47\n"
"v_pk_fma_f32 v[250:251], v[250:251], v[244:245], v[246:247] op_sel_hi:[1,0,0]\n"
"v_cvt_pk_bf16_f32 v47, v250, v251\n"
"ds_write_b128 v7, v[44:47] offset:1152\n"
"s_waitcnt lgkmcnt(0)\n"
"s_setprio 2\n"
"v_mov_b32 v244, 161880\n"
"v_mov_b32 v245, 1\n"
"s_mov_b64 exec, 1\n"
"ds_add_u32 v244, v245\n"
"s_mov_b64 exec, -1\n"
"s_bitcmp1_b32 s31, 0\n"
"s_cbranch_scc1 L_nokv\n"
"s_setprio 3\n"
"v_bfe_u32 v244, v1, 2, 2\n"
"v_lshl_add_u32 v244, v3, 3, v244\n"
"v_mul_u32_u24 v4, 40, v244\n"
"v_bfe_u32 v244, v1, 4, 1\n"
"v_lshl_add_u32 v4, v244, 5, v4\n"
"v_and_b32 v244, 3, v1\n"
"v_lshl_add_u32 v4, v244, 3, v4\n"
"v_add_u32 v4, s35, v4\n"
"s_lshr_b32 s60, s31, 2\n"
"s_lshl_b32 s60, s60, 5\n"
"v_add_u32 v5, s60, v2\n"
"v_mul_u32_u24 v5, 1296, v5\n"
"v_lshl_add_u32 v5, v3, 4, v5\n"
"s_mul_i32 s60, s7, 320\n"
"s_add_u32 s60, s60, 78848\n"
"v_add_u32 v5, s60, v5\n"
"s_waitcnt vmcnt(0)\n"
"v_mov_b32 v244, 161880\n"
"s_mov_b32 s69, 0\n"
"L_sp1:\n"
"ds_read_b32 v245, v244\n"
"s_waitcnt lgkmcnt(0)\n"
"v_cmp_gt_u32 vcc, 4, v245\n"
"s_cbranch_vccz L_spd1\n"
"s_sleep 1\n"
"s_add_u32 s69, s69, 1\n"
"s_cmp_lt_u32 s69, 100000\n"
"s_cbranch_scc1 L_sp1\n"
"L_spd1:\n"
"s_bitcmp1_b32 s31, 1\n"
"s_cbranch_scc1 L_kvV\n"
"ds_read_b64_tr_b16 v[48:49], v4 offset:0\n"
"ds_read_b64_tr_b16 v[50:51], v4 offset:160\n"
"ds_read_b128 v[88:91], v5 offset:0\n"
"ds_read_b64_tr_b16 v[52:53], v4 offset:640\n"
"ds_read_b64_tr_b16 v[54:55], v4 offset:800\n"
"ds_read_b128 v[92:95], v5 offset:32\n"
"ds_read_b64_tr_b16 v[56:57], v4 offset:1280\n"
"ds_read_b64_tr_b16 v[58:59], v4 offset:1440\n"
"ds_read_b128 v[96:99], v5 offset:64\n"
"ds_read_b64_tr_b16 v[60:61], v4 offset:1920\n"
"ds_read_b64_tr_b16 v[62:63], v4 offset:2080\n"
"ds_read_b128 v[100:103], v5 offset:96\n"
"s_waitcnt lgkmcnt(9)\n"
"ds_read_b64_tr_b16 v[64:65], v4 offset:2560\n"
"ds_read_b64_tr_b16 v[66:67], v4 offset:2720\n"
"ds_read_b128 v[104:107], v5 offset:128\n"
"v_mfma_f32_32x32x16_bf16 v[128:143], v[48:51], v[88:91], 0\n"
"s_waitcnt lgkmcnt(9)\n"
"ds_read_b64_tr_b16 v[68:69], v4 offset:3200\n"
"ds_read_b64_tr_b16 v[70:71], v4 offset:3360\n"
"ds_read_b128 v[108:111], v5 offset:160\n"
"v_mfma_f32_32x32x16_bf16 v[128:143], v[52:55], v[92:95], v[128:143]\n"
"s_waitcnt lgkmcnt(9)\n"
"ds_read_b64_tr_b16 v[72:73], v4 offset:3840\n"
"ds_read_b64_tr_b16 v[74:75], v4 offset:4000\n"
"ds_read_b128 v[112:115], v5 offset:192\n"
"v_mfma_f32_32x32x16_bf16 v[128:143], v[56:59], v[96:99], v[128:143]\n"
"s_waitcnt lgkmcnt(9)\n"
"ds_read_b64_tr_b16 v[76:77], v4 offset:4480\n"
"ds_read_b64_tr_b16 v[78:79], v4 offset:4640\n"
"ds_read_b128 v[116:119], v5 offset:224\n"
"v_mfma_f32_32x32x16_bf16 v[128:143], v[60:63], v[100:103], v[128:143]\n"
"s_waitcnt lgkmcnt(9)\n"
"ds_read_b64_tr_b16 v[80:81], v4 offset:5120\n"
"ds_read_b64_tr_b16 v[82:83], v4 offset:5280\n"
"ds_read_b128 v[120:123], v5 offset:256\n"
"v_mfma_f32_32x32x16_bf16 v[128:143], v[64:67], v[104:107], v[128:143]\n"
"s_waitcnt lgkmcnt(9)\n"
"ds_read_b64_tr_b16 v[84:85], v4 offset:5760\n"
"ds_read_b64_tr_b16 v[86:87], v4 offset:5920\n"
"ds_read_b128 v[124:127], v5 offset:288\n"
"v_mfma_f32_32x32x16_bf16 v[128:143], v[68:71], v[108:111], v[128:143]\n"
"s_waitcnt lgkmcnt(9)\n"
"v_mfma_f32_32x32x16_bf16 v[128:143], v[72:75], v[112:115], v[128:143]\n"
"s_waitcnt lgkmcnt(6)\n"
"v_mfma_f32_32x32x16_bf16 v[128:143], v[76:79], v[116:119], v[128:143]\n"
"s_waitcnt lgkmcnt(3)\n"
"v_mfma_f32_32x32x16_bf16 v[128:143], v[80:83], v[120:123], v[128:143]\n"
"s_waitcnt lgkmcnt(0)\n"
"v_mfma_f32_32x32x16_bf16 v[128:143], v[84:87], v[124:127], v[128:143]\n"
"s_branch L_kvJ\n"
"L_kvV:\n"
"ds_read_b64_tr_b16 v[48:49], v4 offset:0\n"
"ds_read_b64_tr_b16 v[50:51], v4 offset:160\n"
"ds_read_b128 v[88:91], v5 offset:0\n"
"ds_read_b64_tr_b16 v[52:53], v4 offset:640\n"
"ds_read_b64_tr_b16 v[54:55], v4 offset:800\n"
"ds_read_b128 v[92:95], v5 offset:32\n"
"ds_read_b64_tr_b16 v[56:57], v4 offset:1280\n"
"ds_read_b64_tr_b16 v[58:59], v4 offset:1440\n"
"ds_read_b128 v[96:99], v5 offset:64\n"
"ds_read_b64_tr_b16 v[60:61], v4 offset:1920\n"
"ds_read_b64_tr_b16 v[62:63], v4 offset:2080\n"
"ds_read_b128 v[100:103], v5 offset:96\n"
"s_waitcnt lgkmcnt(9)\n"
"ds_read_b64_tr_b16 v[64:65], v4 offset:2560\n"
"ds_read_b64_tr_b16 v[66:67], v4 offset:2720\n"
"ds_read_b128 v[104:107], v5 offset:128\n"
"v_mfma_f32_32x32x16_bf16 v[128:143], v[88:91], v[48:51], 0\n"
"s_waitcnt lgkmcnt(9)\n"
"ds_read_b64_tr_b16 v[68:69], v4 offset:3200\n"
"ds_read_b64_tr_b16 v[70:71], v4 offset:3360\n"
"ds_read_b128 v[108:111], v5 offset:160\n"
"v_mfma_f32_32x32x16_bf16 v[128:143], v[92:95], v[52:55], v[128:143]\n"
"s_waitcnt lgkmcnt(9)\n"
"ds_read_b64_tr_b16 v[72:73], v4 offset:3840\n"
"ds_read_b64_tr_b16 v[74:75], v4 offset:4000\n"
"ds_read_b128 v[112:115], v5 offset:192\n"
"v_mfma_f32_32x32x16_bf16 v[128:143], v[96:99], v[56:59], v[128:143]\n"
"s_waitcnt lgkmcnt(9)\n"
"ds_read_b64_tr_b16 v[76:77], v4 offset:4480\n"
"ds_read_b64_tr_b16 v[78:79], v4 offset:4640\n"
"ds_read_b128 v[116:119], v5 offset:224\n"
"v_mfma_f32_32x32x16_bf16 v[128:143], v[100:103], v[60:63], v[128:143]\n"
"s_waitcnt lgkmcnt(9)\n"
"ds_read_b64_tr_b16 v[80:81], v4 offset:5120\n"
"ds_read_b64_tr_b16 v[82:83], v4 offset:5280\n"
"ds_read_b128 v[120:123], v5 offset:256\n"
"v_mfma_f32_32x32x16_bf16 v[128:143], v[104:107], v[64:67], v[128:143]\n"
"s_waitcnt lgkmcnt(9)\n"
"ds_read_b64_tr_b16 v[84:85], v4 offset:5760\n"
"ds_read_b64_tr_b16 v[86:87], v4 offset:5920\n"
"ds_read_b128 v[124:127], v5 offset:288\n"
"v_mfma_f32_32x32x16_bf16 v[128:143], v[108:111], v[68:71], v[128:143]\n"
"s_waitcnt lgkmcnt(9)\n"
"v_mfma_f32_32x32x16_bf16 v[128:143], v[112:115], v[72:75], v[128:143]\n"
"s_waitcnt lgkmcnt(6)\n"
"v_mfma_f32_32x32x16_bf16 v[128:143], v[116:119], v[76:79], v[128:143]\n"
"s_waitcnt lgkmcnt(3)\n"
"v_mfma_f32_32x32x16_bf16 v[128:143], v[120:123], v[80:83], v[128:143]\n"
"s_waitcnt lgkmcnt(0)\n"
"v_mfma_f32_32x32x16_bf16 v[128:143], v[124:127], v[84:87], v[128:143]\n"
"L_kvJ:\n"
"v_lshlrev_b32 v6, 4, v1\n"
"s_cmp_eq_u32 s7, 0\n"
"s_cbranch_scc1 L_kvR\n"
"v_add_u32 v7, s35, v6\n"
"s_nop 6\n"
"s_nop 0\n"
"ds_write_b128 v7, v[128:131] offset:0\n"
"ds_write_b128 v7, v[132:135] offset:1024\n"
"ds_write_b128 v7, v[136:139] offset:2048\n"
"ds_write_b128 v7, v[140:143] offset:3072\n"
"v_mov_b32 v244, 161808\n"
"v_mov_b32 v245, 1\n"
"s_mov_b64 exec, 1\n"
"ds_add_u32 v244, v245\n"
"s_mov_b64 exec, -1\n"
"s_branch L_nokv\n"
"L_kvR:\n"
"v_mov_b32 v244, 161808\n"
"s_mov_b32 s69, 0\n"
"L_sp2:\n"
"ds_read_b32 v245, v244\n"
"s_waitcnt lgkmcnt(0)\n"
"v_cmp_gt_u32 vcc, 3, v245\n"
"s_cbranch_vccz L_spd2\n"
"s_sleep 1\n"
"s_add_u32 s69, s69, 1\n"
"s_cmp_lt_u32 s69, 100000\n"
"s_cbranch_scc1 L_sp2\n"
"L_spd2:\n"
"v_add_u32 v7, 53248, v6\n"
"ds_read_b128 v[144:147], v7 offset:6400\n"
"ds_read_b128 v[148:151], v7 offset:7424\n"
"ds_read_b128 v[152:155], v7 offset:8448\n"
"ds_read_b128 v[156:159], v7 offset:9472\n"
"ds_read_b128 v[160:163], v7 offset:12800\n"
"ds_read_b128 v[164:167], v7 offset:13824\n"
"ds_read_b128 v[168:171], v7 offset:14848\n"
"ds_read_b128 v[172:175], v7 offset:15872\n"
"ds_read_b128 v[176:179], v7 offset:19200\n"
"ds_read_b128 v[180:183], v7 offset:20224\n"
"ds_read_b128 v[184:187], v7 offset:21248\n"
"ds_read_b128 v[188:191], v7 offset:22272\n"
"s_waitcnt lgkmcnt(0)\n"
"v_mov_b32 v244, 161856\n"
"v_mov_b32 v245, 1\n"
"s_mov_b64 exec, 1\n"
"ds_add_u32 v244, v245\n"
"s_mov_b64 exec, -1\n"
"v_add_f32 v128, v128, v144\n"
"v_add_f32 v129, v129, v145\n"
"v_add_f32 v130, v130, v146\n"
"v_add_f32 v131, v131, v147\n"
"v_add_f32 v132, v132, v148\n"
"v_add_f32 v133, v133, v149\n"
"v_add_f32 v134, v134, v150\n"
"v_add_f32 v135, v135, v151\n"
"v_add_f32 v136, v136, v152\n"
"v_add_f32 v137, v137, v153\n"
"v_add_f32 v138, v138, v154\n"
"v_add_f32 v139, v139, v155\n"
"v_add_f32 v140, v140, v156\n"
"v_add_f32 v141, v141, v157\n"
"v_add_f32 v142, v142, v158\n"
"v_add_f32 v143, v143, v159\n"
"v_add_f32 v128, v128, v160\n"
"v_add_f32 v129, v129, v161\n"
"v_add_f32 v130, v130, v162\n"
"v_add_f32 v131, v131, v163\n"
"v_add_f32 v132, v132, v164\n"
"v_add_f32 v133, v133, v165\n"
"v_add_f32 v134, v134, v166\n"
"v_add_f32 v135, v135, v167\n"
"v_add_f32 v136, v136, v168\n"
"v_add_f32 v137, v137, v169\n"
"v_add_f32 v138, v138, v170\n"
"v_add_f32 v139, v139, v171\n"
"v_add_f32 v140, v140, v172\n"
"v_add_f32 v141, v141, v173\n"
"v_add_f32 v142, v142, v174\n"
"v_add_f32 v143, v143, v175\n"
"v_add_f32 v128, v128, v176\n"
"v_add_f32 v129, v129, v177\n"
"v_add_f32 v130, v130, v178\n"
"v_add_f32 v131, v131, v179\n"
"v_add_f32 v132, v132, v180\n"
"v_add_f32 v133, v133, v181\n"
"v_add_f32 v134, v134, v182\n"
"v_add_f32 v135, v135, v183\n"
"v_add_f32 v136, v136, v184\n"
"v_add_f32 v137, v137, v185\n"
"v_add_f32 v138, v138, v186\n"
"v_add_f32 v139, v139, v187\n"
"v_add_f32 v140, v140, v188\n"
"v_add_f32 v141, v141, v189\n"
"v_add_f32 v142, v142, v190\n"
"v_add_f32 v143, v143, v191\n"
"v_cvt_pk_bf16_f32 v240, v128, v129\n"
"v_cvt_pk_bf16_f32 v241, v130, v131\n"
"v_cvt_pk_bf16_f32 v242, v132, v133\n"
"v_cvt_pk_bf16_f32 v243, v134, v135\n"
"v_cvt_pk_bf16_f32 v248, v136, v137\n"
"v_cvt_pk_bf16_f32 v249, v138, v139\n"
"v_cvt_pk_bf16_f32 v250, v140, v141\n"
"v_cvt_pk_bf16_f32 v251, v142, v143\n"
"s_lshl_b32 s60, s30, 1\n"
"s_lshr_b32 s61, s31, 2\n"
"s_add_u32 s60, s60, s61\n"
"s_mul_i32 s60, s60, 2624\n"
"s_add_u32 s60, s60, 65536\n"
"s_bitcmp1_b32 s31, 1\n"
"s_cbranch_scc1 L_kvSV\n"
"v_lshlrev_b32 v6, 4, v2\n"
"v_lshl_add_u32 v6, v3, 9, v6\n"
"buffer_store_dwordx4 v[240:243], v6, s[36:39], s60 offen sc1\n"
"v_lshlrev_b32 v7, 3, v2\n"
"s_mov_b32 exec_hi, 0\n"
"buffer_store_dwordx2 v[248:249], v7, s[36:39], s60 offen offset:1024 sc1\n"
"s_mov_b64 exec, -1\n"
"s_branch L_kvSJ\n"
"L_kvSV:\n"
"v_cmp_eq_u32 vcc, 20, v2\n"
"v_mov_b32 v7, 0x3f803f80\n"
"s_nop 0\n"
"v_cndmask_b32 v240, v240, v7, vcc\n"
"v_cndmask_b32 v241, v241, v7, vcc\n"
"v_cndmask_b32 v242, v242, v7, vcc\n"
"v_cndmask_b32 v243, v243, v7, vcc\n"
"v_cndmask_b32 v248, v248, v7, vcc\n"
"v_cndmask_b32 v249, v249, v7, vcc\n"
"v_cndmask_b32 v250, v250, v7, vcc\n"
"v_cndmask_b32 v251, v251, v7, vcc\n"
"v_mul_u32_u24 v6, 336, v3\n"
"v_lshl_add_u32 v6, v2, 4, v6\n"
"s_mov_b32 exec_lo, 0x1fffff\n"
"s_mov_b32 exec_hi, 0x1fffff\n"
"buffer_store_dwordx4 v[240:243], v6, s[36:39], s60 offen offset:1280 sc1\n"
"buffer_store_dwordx4 v[248:251], v6, s[36:39], s60 offen offset:1952 sc1\n"
"s_mov_b64 exec, -1\n"
"L_kvSJ:\n"
"s_mov_b32 s34, 1\n"
"s_branch L_kvdone\n"
"L_nokv:\n"
"s_mov_b32 s34, 0\n"
"L_kvdone:\n"
"s_setprio 2\n"
"v_bfe_u32 v244, v1, 2, 2\n"
"v_lshlrev_b32 v244, 1, v244\n"
"v_lshl_add_u32 v244, v3, 3, v244\n"
"v_mul_u32_u24 v4, 160, v244\n"
"v_bfe_u32 v244, v1, 4, 1\n"
"v_lshl_add_u32 v4, v244, 5, v4\n"
"v_and_b32 v244, 3, v1\n"
"v_lshl_add_u32 v4, v244, 3, v4\n"
"s_mul_i32 s60, s7, 40\n"
"v_add_u32 v4, s60, v4\n"
"v_mul_u32_u24 v6, 1296, v2\n"
"v_lshl_add_u32 v6, v3, 4, v6\n"
"v_add_u32 v6, 78848, v6\n"
"v_mov_b32 v244, 161880\n"
"s_mov_b32 s69, 0\n"
"L_sp3:\n"
"ds_read_b32 v245, v244\n"
"s_waitcnt lgkmcnt(0)\n"
"v_cmp_gt_u32 vcc, 4, v245\n"
"s_cbranch_vccz L_spd3\n"
"s_sleep 1\n"
"s_add_u32 s69, s69, 1\n"
"s_cmp_lt_u32 s69, 100000\n"
"s_cbranch_scc1 L_sp3\n"
"L_spd3:\n"
"v_mov_b32 v244, 161912\n"
"s_mov_b32 s69, 0\n"
"L_sp4:\n"
"ds_read_b32 v245, v244\n"
"s_waitcnt lgkmcnt(0)\n"
"v_cmp_gt_u32 vcc, 4, v245\n"
"s_cbranch_vccz L_spd4\n"
"s_sleep 1\n"
"s_add_u32 s69, s69, 1\n"
"s_cmp_lt_u32 s69, 100000\n"
"s_cbranch_scc1 L_sp4\n"
"L_spd4:\n"
"v_mov_b32 v244, 161860\n"
"s_mov_b32 s69, 0\n"
"L_sp5:\n"
"ds_read_b32 v245, v244\n"
"s_waitcnt lgkmcnt(0)\n"
"v_cmp_gt_u32 vcc, 4, v245\n"
"s_cbranch_vccz L_spd5\n"
"s_sleep 1\n"
"s_add_u32 s69, s69, 1\n"
"s_cmp_lt_u32 s69, 100000\n"
"s_cbranch_scc1 L_sp5\n"
"L_spd5:\n"
"ds_read_b64_tr_b16 v[48:49], v4 offset:0\n"
"ds_read_b64_tr_b16 v[50:51], v4 offset:160\n"
"ds_read_b64_tr_b16 v[52:53], v4 offset:2560\n"
"ds_read_b64_tr_b16 v[54:55], v4 offset:2720\n"
"ds_read_b64_tr_b16 v[56:57], v4 offset:5120\n"
"ds_read_b64_tr_b16 v[58:59], v4 offset:5280\n"
"ds_read_b64_tr_b16 v[60:61], v4 offset:7680\n"
"ds_read_b64_tr_b16 v[62:63], v4 offset:7840\n"
"ds_read_b64_tr_b16 v[64:65], v4 offset:10240\n"
"ds_read_b64_tr_b16 v[66:67], v4 offset:10400\n"
"ds_read_b64_tr_b16 v[68:69], v4 offset:12800\n"
"ds_read_b64_tr_b16 v[70:71], v4 offset:12960\n"
"s_waitcnt lgkmcnt(2)\n"
"ds_read_b64_tr_b16 v[72:73], v4 offset:15360\n"
"ds_read_b64_tr_b16 v[74:75], v4 offset:15520\n"
"ds_read_b64_tr_b16 v[76:77], v4 offset:17920\n"
"ds_read_b64_tr_b16 v[78:79], v4 offset:18080\n"
"ds_read_b64_tr_b16 v[80:81], v4 offset:20480\n"
"ds_read_b64_tr_b16 v[82:83], v4 offset:20640\n"
"ds_read_b64_tr_b16 v[84:85], v4 offset:23040\n"
"ds_read_b64_tr_b16 v[86:87], v4 offset:23200\n"
"ds_read_b64_tr_b16 v[88:89], v4 offset:25600\n"
"ds_read_b64_tr_b16 v[90:91], v4 offset:25760\n"
"ds_read_b64_tr_b16 v[92:93], v4 offset:28160\n"
"ds_read_b64_tr_b16 v[94:95], v4 offset:28320\n"
"s_waitcnt lgkmcnt(2)\n"
"ds_read_b64_tr_b16 v[96:97], v4 offset:30720\n"
"ds_read_b64_tr_b16 v[98:99], v4 offset:30880\n"
"ds_read_b64_tr_b16 v[100:101], v4 offset:33280\n"
"ds_read_b64_tr_b16 v[102:103], v4 offset:33440\n"
"ds_read_b64_tr_b16 v[104:105], v4 offset:35840\n"
"ds_read_b64_tr_b16 v[106:107], v4 offset:36000\n"
"ds_read_b64_tr_b16 v[108:109], v4 offset:38400\n"
"ds_read_b64_tr_b16 v[110:111], v4 offset:38560\n"
"s_waitcnt lgkmcnt(0)\n"
"v_mov_b32 v244, 161864\n"
"v_mov_b32 v245, 1\n"
"s_mov_b64 exec, 1\n"
"ds_add_u32 v244, v245\n"
"s_mov_b64 exec, -1\n"
"ds_read_b128 v[8:11], v6 offset:0\n"
"ds_read_b128 v[12:15], v6 offset:41472\n"
"ds_read_b128 v[16:19], v6 offset:32\n"
"ds_read_b128 v[20:23], v6 offset:41504\n"
"s_waitcnt lgkmcnt(2)\n"
"v_mfma_f32_32x32x16_bf16 v[208:223], v[48:51], v[8:11], 0\n"
"v_mfma_f32_32x32x16_bf16 v[224:239], v[48:51], v[12:15], 0\n"
"ds_read_b128 v[24:27], v6 offset:64\n"
"ds_read_b128 v[28:31], v6 offset:41536\n"
"s_waitcnt lgkmcnt(2)\n"
"v_mfma_f32_32x32x16_bf16 v[208:223], v[52:55], v[16:19], v[208:223]\n"
"v_mfma_f32_32x32x16_bf16 v[224:239], v[52:55], v[20:23], v[224:239]\n"
"ds_read_b128 v[32:35], v6 offset:96\n"
"ds_read_b128 v[36:39], v6 offset:41568\n"
"s_waitcnt lgkmcnt(2)\n"
"v_mfma_f32_32x32x16_bf16 v[208:223], v[56:59], v[24:27], v[208:223]\n"
"v_mfma_f32_32x32x16_bf16 v[224:239], v[56:59], v[28:31], v[224:239]\n"
"ds_read_b128 v[40:43], v6 offset:128\n"
"ds_read_b128 v[44:47], v6 offset:41600\n"
"s_waitcnt lgkmcnt(2)\n"
"v_mfma_f32_32x32x16_bf16 v[208:223], v[60:63], v[32:35], v[208:223]\n"
"v_mfma_f32_32x32x16_bf16 v[224:239], v[60:63], v[36:39], v[224:239]\n"
"ds_read_b128 v[8:11], v6 offset:160\n"
"ds_read_b128 v[12:15], v6 offset:41632\n"
"s_waitcnt lgkmcnt(2)\n"
"v_mfma_f32_32x32x16_bf16 v[208:223], v[64:67], v[40:43], v[208:223]\n"
"v_mfma_f32_32x32x16_bf16 v[224:239], v[64:67], v[44:47], v[224:239]\n"
"ds_read_b128 v[16:19], v6 offset:192\n"
"ds_read_b128 v[20:23], v6 offset:41664\n"
"s_waitcnt lgkmcnt(2)\n"
"v_mfma_f32_32x32x16_bf16 v[208:223], v[68:71], v[8:11], v[208:223]\n"
"v_mfma_f32_32x32x16_bf16 v[224:239], v[68:71], v[12:15], v[224:239]\n"
"s_cmp_eq_u32 s34, 0\n"
"s_cbranch_scc1 L_kvpub6\n"
"s_waitcnt vmcnt(0)\n"
"s_lshl_b32 s60, s30, 2\n"
"s_lshr_b32 s61, s31, 1\n"
"s_add_u32 s60, s60, s61\n"
"s_lshl_b32 s60, s60, 3\n"
"s_add_u32 s60, s60, 262144\n"
"v_mov_b32 v246, s32\n"
"v_mov_b32 v247, s33\n"
"v_mul_u32_u24 v244, 1024, v1\n"
"s_mov_b64 exec, 0xffffffff\n"
"buffer_store_dwordx2 v[246:247], v244, s[36:39], s60 offen sc1\n"
"s_mov_b64 exec, -1\n"
"L_kvpub6:\n"
"ds_read_b128 v[24:27], v6 offset:224\n"
"ds_read_b128 v[28:31], v6 offset:41696\n"
"s_waitcnt lgkmcnt(2)\n"
"v_mfma_f32_32x32x16_bf16 v[208:223], v[72:75], v[16:19], v[208:223]\n"
"v_mfma_f32_32x32x16_bf16 v[224:239], v[72:75], v[20:23], v[224:239]\n"
"ds_read_b128 v[32:35], v6 offset:256\n"
"ds_read_b128 v[36:39], v6 offset:41728\n"
"s_waitcnt lgkmcnt(2)\n"
"v_mfma_f32_32x32x16_bf16 v[208:223], v[76:79], v[24:27], v[208:223]\n"
"v_mfma_f32_32x32x16_bf16 v[224:239], v[76:79], v[28:31], v[224:239]\n"
"ds_read_b128 v[40:43], v6 offset:288\n"
"ds_read_b128 v[44:47], v6 offset:41760\n"
"s_waitcnt lgkmcnt(2)\n"
"v_mfma_f32_32x32x16_bf16 v[208:223], v[80:83], v[32:35], v[208:223]\n"
"v_mfma_f32_32x32x16_bf16 v[224:239], v[80:83], v[36:39], v[224:239]\n"
"ds_read_b128 v[8:11], v6 offset:320\n"
"ds_read_b128 v[12:15], v6 offset:41792\n"
"s_waitcnt lgkmcnt(2)\n"
"v_mfma_f32_32x32x16_bf16 v[208:223], v[84:87], v[40:43], v[208:223]\n"
"v_mfma_f32_32x32x16_bf16 v[224:239], v[84:87], v[44:47], v[224:239]\n"
"ds_read_b128 v[16:19], v6 offset:352\n"
"ds_read_b128 v[20:23], v6 offset:41824\n"
"s_waitcnt lgkmcnt(2)\n"
"v_mfma_f32_32x32x16_bf16 v[208:223], v[88:91], v[8:11], v[208:223]\n"
"v_mfma_f32_32x32x16_bf16 v[224:239], v[88:91], v[12:15], v[224:239]\n"
"v_mov_b32 v244, 161868\n"
"s_mov_b32 s69, 0\n"
"L_sp7:\n"
"ds_read_b32 v245, v244\n"
"s_waitcnt lgkmcnt(0)\n"
"v_cmp_gt_u32 vcc, 4, v245\n"
"s_cbranch_vccz L_spd7\n"
"s_sleep 1\n"
"s_add_u32 s69, s69, 1\n"
"s_cmp_lt_u32 s69, 100000\n"
"s_cbranch_scc1 L_sp7\n"
"L_spd7:\n"
"ds_read_b128 v[24:27], v6 offset:384\n"
"ds_read_b128 v[28:31], v6 offset:41856\n"
"ds_read_b64_tr_b16 v[112:113], v4 offset:0\n"
"ds_read_b64_tr_b16 v[114:115], v4 offset:160\n"
"ds_read_b64_tr_b16 v[116:117], v4 offset:2560\n"
"ds_read_b64_tr_b16 v[118:119], v4 offset:2720\n"
"ds_read_b64_tr_b16 v[120:121], v4 offset:5120\n"
"ds_read_b64_tr_b16 v[122:123], v4 offset:5280\n"
"ds_read_b64_tr_b16 v[124:125], v4 offset:7680\n"
"ds_read_b64_tr_b16 v[126:127], v4 offset:7840\n"
"ds_read_b64_tr_b16 v[128:129], v4 offset:10240\n"
"ds_read_b64_tr_b16 v[130:131], v4 offset:10400\n"
"ds_read_b64_tr_b16 v[132:133], v4 offset:12800\n"
"ds_read_b64_tr_b16 v[134:135], v4 offset:12960\n"
"s_waitcnt lgkmcnt(8)\n"
"v_mfma_f32_32x32x16_bf16 v[208:223], v[92:95], v[16:19], v[208:223]\n"
"v_mfma_f32_32x32x16_bf16 v[224:239], v[92:95], v[20:23], v[224:239]\n"
"ds_read_b128 v[32:35], v6 offset:416\n"
"ds_read_b128 v[36:39], v6 offset:41888\n"
"ds_read_b64_tr_b16 v[136:137], v4 offset:15360\n"
"ds_read_b64_tr_b16 v[138:139], v4 offset:15520\n"
"ds_read_b64_tr_b16 v[140:141], v4 offset:17920\n"
"ds_read_b64_tr_b16 v[142:143], v4 offset:18080\n"
"s_waitcnt lgkmcnt(8)\n"
"v_mfma_f32_32x32x16_bf16 v[208:223], v[96:99], v[24:27], v[208:223]\n"
"v_mfma_f32_32x32x16_bf16 v[224:239], v[96:99], v[28:31], v[224:239]\n"
"ds_read_b128 v[40:43], v6 offset:448\n"
"ds_read_b128 v[44:47], v6 offset:41920\n"
"ds_read_b64_tr_b16 v[144:145], v4 offset:20480\n"
"ds_read_b64_tr_b16 v[146:147], v4 offset:20640\n"
"ds_read_b64_tr_b16 v[148:149], v4 offset:23040\n"
"ds_read_b64_tr_b16 v[150:151], v4 offset:23200\n"
"s_waitcnt lgkmcnt(8)\n"
"v_mfma_f32_32x32x16_bf16 v[208:223], v[100:103], v[32:35], v[208:223]\n"
"v_mfma_f32_32x32x16_bf16 v[224:239], v[100:103], v[36:39], v[224:239]\n"
"ds_read_b128 v[8:11], v6 offset:480\n"
"ds_read_b128 v[12:15], v6 offset:41952\n"
"ds_read_b64_tr_b16 v[152:153], v4 offset:25600\n"
"ds_read_b64_tr_b16 v[154:155], v4 offset:25760\n"
"ds_read_b64_tr_b16 v[156:157], v4 offset:28160\n"
"ds_read_b64_tr_b16 v[158:159], v4 offset:28320\n"
"s_waitcnt lgkmcnt(8)\n"
"v_mfma_f32_32x32x16_bf16 v[208:223], v[104:107], v[40:43], v[208:223]\n"
"v_mfma_f32_32x32x16_bf16 v[224:239], v[104:107], v[44:47], v[224:239]\n"
"ds_read_b128 v[16:19], v6 offset:512\n"
"ds_read_b128 v[20:23], v6 offset:41984\n"
"ds_read_b64_tr_b16 v[160:161], v4 offset:30720\n"
"ds_read_b64_tr_b16 v[162:163], v4 offset:30880\n"
"ds_read_b64_tr_b16 v[164:165], v4 offset:33280\n"
"ds_read_b64_tr_b16 v[166:167], v4 offset:33440\n"
"s_waitcnt lgkmcnt(8)\n"
"v_mfma_f32_32x32x16_bf16 v[208:223], v[108:111], v[8:11], v[208:223]\n"
"v_mfma_f32_32x32x16_bf16 v[224:239], v[108:111], v[12:15], v[224:239]\n"
"ds_read_b128 v[24:27], v6 offset:544\n"
"ds_read_b128 v[28:31], v6 offset:42016\n"
"ds_read_b64_tr_b16 v[168:169], v4 offset:35840\n"
"ds_read_b64_tr_b16 v[170:171], v4 offset:36000\n"
"ds_read_b64_tr_b16 v[172:173], v4 offset:38400\n"
"ds_read_b64_tr_b16 v[174:175], v4 offset:38560\n"
"s_waitcnt lgkmcnt(8)\n"
"v_mfma_f32_32x32x16_bf16 v[208:223], v[112:115], v[16:19], v[208:223]\n"
"v_mfma_f32_32x32x16_bf16 v[224:239], v[112:115], v[20:23], v[224:239]\n"
"ds_read_b128 v[32:35], v6 offset:576\n"
"ds_read_b128 v[36:39], v6 offset:42048\n"
"ds_read_b64_tr_b16 v[176:177], v4 offset:40960\n"
"ds_read_b64_tr_b16 v[178:179], v4 offset:41120\n"
"ds_read_b64_tr_b16 v[180:181], v4 offset:43520\n"
"ds_read_b64_tr_b16 v[182:183], v4 offset:43680\n"
"s_waitcnt lgkmcnt(8)\n"
"v_mfma_f32_32x32x16_bf16 v[208:223], v[116:119], v[24:27], v[208:223]\n"
"v_mfma_f32_32x32x16_bf16 v[224:239], v[116:119], v[28:31], v[224:239]\n"
"ds_read_b128 v[40:43], v6 offset:608\n"
"ds_read_b128 v[44:47], v6 offset:42080\n"
"ds_read_b64_tr_b16 v[184:185], v4 offset:46080\n"
"ds_read_b64_tr_b16 v[186:187], v4 offset:46240\n"
"ds_read_b64_tr_b16 v[188:189], v4 offset:48640\n"
"ds_read_b64_tr_b16 v[190:191], v4 offset:48800\n"
"s_waitcnt lgkmcnt(8)\n"
"v_mfma_f32_32x32x16_bf16 v[208:223], v[120:123], v[32:35], v[208:223]\n"
"v_mfma_f32_32x32x16_bf16 v[224:239], v[120:123], v[36:39], v[224:239]\n"
"ds_read_b128 v[8:11], v6 offset:640\n"
"ds_read_b128 v[12:15], v6 offset:42112\n"
"ds_read_b64_tr_b16 v[192:193], v4 offset:51200\n"
"ds_read_b64_tr_b16 v[194:195], v4 offset:51360\n"
"ds_read_b64_tr_b16 v[196:197], v4 offset:53760\n"
"ds_read_b64_tr_b16 v[198:199], v4 offset:53920\n"
"s_waitcnt lgkmcnt(8)\n"
"v_mfma_f32_32x32x16_bf16 v[208:223], v[124:127], v[40:43], v[208:223]\n"
"v_mfma_f32_32x32x16_bf16 v[224:239], v[124:127], v[44:47], v[224:239]\n"
"ds_read_b128 v[16:19], v6 offset:672\n"
"ds_read_b128 v[20:23], v6 offset:42144\n"
"ds_read_b64_tr_b16 v[200:201], v4 offset:56320\n"
"ds_read_b64_tr_b16 v[202:203], v4 offset:56480\n"
"ds_read_b64_tr_b16 v[204:205], v4 offset:58880\n"
"ds_read_b64_tr_b16 v[206:207], v4 offset:59040\n"
"s_waitcnt lgkmcnt(10)\n"
"v_mfma_f32_32x32x16_bf16 v[208:223], v[128:131], v[8:11], v[208:223]\n"
"v_mfma_f32_32x32x16_bf16 v[224:239], v[128:131], v[12:15], v[224:239]\n"
"ds_read_b128 v[24:27], v6 offset:704\n"
"ds_read_b128 v[28:31], v6 offset:42176\n"
"s_waitcnt lgkmcnt(6)\n"
"v_mfma_f32_32x32x16_bf16 v[208:223], v[132:135], v[16:19], v[208:223]\n"
"v_mfma_f32_32x32x16_bf16 v[224:239], v[132:135], v[20:23], v[224:239]\n"
"ds_read_b128 v[32:35], v6 offset:736\n"
"ds_read_b128 v[36:39], v6 offset:42208\n"
"s_waitcnt lgkmcnt(2)\n"
"v_mfma_f32_32x32x16_bf16 v[208:223], v[136:139], v[24:27], v[208:223]\n"
"v_mfma_f32_32x32x16_bf16 v[224:239], v[136:139], v[28:31], v[224:239]\n"
"v_mov_b32 v244, 161916\n"
"v_mov_b32 v245, 1\n"
"s_mov_b64 exec, 1\n"
"ds_add_u32 v244, v245\n"
"s_mov_b64 exec, -1\n"
"ds_read_b128 v[40:43], v6 offset:768\n"
"ds_read_b128 v[44:47], v6 offset:42240\n"
"v_mov_b32 v244, 161872\n"
"v_mov_b32 v245, 1\n"
"s_mov_b64 exec, 1\n"
"ds_add_u32 v244, v245\n"
"s_mov_b64 exec, -1\n"
"s_waitcnt lgkmcnt(2)\n"
"v_mfma_f32_32x32x16_bf16 v[208:223], v[140:143], v[32:35], v[208:223]\n"
"v_mfma_f32_32x32x16_bf16 v[224:239], v[140:143], v[36:39], v[224:239]\n"
"ds_read_b128 v[8:11], v6 offset:800\n"
"ds_read_b128 v[12:15], v6 offset:42272\n"
"s_waitcnt lgkmcnt(2)\n"
"v_mfma_f32_32x32x16_bf16 v[208:223], v[144:147], v[40:43], v[208:223]\n"
"v_mfma_f32_32x32x16_bf16 v[224:239], v[144:147], v[44:47], v[224:239]\n"
"ds_read_b128 v[16:19], v6 offset:832\n"
"ds_read_b128 v[20:23], v6 offset:42304\n"
"s_waitcnt lgkmcnt(2)\n"
"v_mfma_f32_32x32x16_bf16 v[208:223], v[148:151], v[8:11], v[208:223]\n"
"v_mfma_f32_32x32x16_bf16 v[224:239], v[148:151], v[12:15], v[224:239]\n"
"ds_read_b128 v[24:27], v6 offset:864\n"
"ds_read_b128 v[28:31], v6 offset:42336\n"
"s_waitcnt lgkmcnt(2)\n"
"v_mfma_f32_32x32x16_bf16 v[208:223], v[152:155], v[16:19], v[208:223]\n"
"v_mfma_f32_32x32x16_bf16 v[224:239], v[152:155], v[20:23], v[224:239]\n"
"ds_read_b128 v[32:35], v6 offset:896\n"
"ds_read_b128 v[36:39], v6 offset:42368\n"
"s_waitcnt lgkmcnt(2)\n"
"v_mfma_f32_32x32x16_bf16 v[208:223], v[156:159], v[24:27], v[208:223]\n"
"v_mfma_f32_32x32x16_bf16 v[224:239], v[156:159], v[28:31], v[224:239]\n"
"ds_read_b128 v[40:43], v6 offset:928\n"
"ds_read_b128 v[44:47], v6 offset:42400\n"
"s_waitcnt lgkmcnt(2)\n"
"v_mfma_f32_32x32x16_bf16 v[208:223], v[160:163], v[32:35], v[208:223]\n"
"v_mfma_f32_32x32x16_bf16 v[224:239], v[160:163], v[36:39], v[224:239]\n"
"ds_read_b128 v[8:11], v6 offset:960\n"
"ds_read_b128 v[12:15], v6 offset:42432\n"
"s_waitcnt lgkmcnt(2)\n"
"v_mfma_f32_32x32x16_bf16 v[208:223], v[164:167], v[40:43], v[208:223]\n"
"v_mfma_f32_32x32x16_bf16 v[224:239], v[164:167], v[44:47], v[224:239]\n"
"ds_read_b128 v[16:19], v6 offset:992\n"
"ds_read_b128 v[20:23], v6 offset:42464\n"
"s_waitcnt lgkmcnt(2)\n"
"v_mfma_f32_32x32x16_bf16 v[208:223], v[168:171], v[8:11], v[208:223]\n"
"v_mfma_f32_32x32x16_bf16 v[224:239], v[168:171], v[12:15], v[224:239]\n"
"ds_read_b128 v[24:27], v6 offset:1024\n"
"ds_read_b128 v[28:31], v6 offset:42496\n"
"s_waitcnt lgkmcnt(2)\n"
"v_mfma_f32_32x32x16_bf16 v[208:223], v[172:175], v[16:19], v[208:223]\n"
"v_mfma_f32_32x32x16_bf16 v[224:239], v[172:175], v[20:23], v[224:239]\n"
"ds_read_b128 v[32:35], v6 offset:1056\n"
"ds_read_b128 v[36:39], v6 offset:42528\n"
"s_waitcnt lgkmcnt(2)\n"
"v_mfma_f32_32x32x16_bf16 v[208:223], v[176:179], v[24:27], v[208:223]\n"
"v_mfma_f32_32x32x16_bf16 v[224:239], v[176:179], v[28:31], v[224:239]\n"
"ds_read_b128 v[40:43], v6 offset:1088\n"
"ds_read_b128 v[44:47], v6 offset:42560\n"
"s_waitcnt lgkmcnt(2)\n"
"v_mfma_f32_32x32x16_bf16 v[208:223], v[180:183], v[32:35], v[208:223]\n"
"v_mfma_f32_32x32x16_bf16 v[224:239], v[180:183], v[36:39], v[224:239]\n"
"ds_read_b128 v[8:11], v6 offset:1120\n"
"ds_read_b128 v[12:15], v6 offset:42592\n"
"s_waitcnt lgkmcnt(2)\n"
"v_mfma_f32_32x32x16_bf16 v[208:223], v[184:187], v[40:43], v[208:223]\n"
"v_mfma_f32_32x32x16_bf16 v[224:239], v[184:187], v[44:47], v[224:239]\n"
"ds_read_b128 v[16:19], v6 offset:1152\n"
"ds_read_b128 v[20:23], v6 offset:42624\n"
"s_waitcnt lgkmcnt(2)\n"
"v_mfma_f32_32x32x16_bf16 v[208:223], v[188:191], v[8:11], v[208:223]\n"
"v_mfma_f32_32x32x16_bf16 v[224:239], v[188:191], v[12:15], v[224:239]\n"
"ds_read_b128 v[24:27], v6 offset:1184\n"
"ds_read_b128 v[28:31], v6 offset:42656\n"
"s_waitcnt lgkmcnt(2)\n"
"v_mfma_f32_32x32x16_bf16 v[208:223], v[192:195], v[16:19], v[208:223]\n"
"v_mfma_f32_32x32x16_bf16 v[224:239], v[192:195], v[20:23], v[224:239]\n"
"ds_read_b128 v[32:35], v6 offset:1216\n"
"ds_read_b128 v[36:39], v6 offset:42688\n"
"s_waitcnt lgkmcnt(2)\n"
"v_mfma_f32_32x32x16_bf16 v[208:223], v[196:199], v[24:27], v[208:223]\n"
"v_mfma_f32_32x32x16_bf16 v[224:239], v[196:199], v[28:31], v[224:239]\n"
"ds_read_b128 v[40:43], v6 offset:1248\n"
"ds_read_b128 v[44:47], v6 offset:42720\n"
"s_waitcnt lgkmcnt(2)\n"
"v_mfma_f32_32x32x16_bf16 v[208:223], v[200:203], v[32:35], v[208:223]\n"
"v_mfma_f32_32x32x16_bf16 v[224:239], v[200:203], v[36:39], v[224:239]\n"
"s_waitcnt lgkmcnt(0)\n"
"v_mfma_f32_32x32x16_bf16 v[208:223], v[204:207], v[40:43], v[208:223]\n"
"v_mfma_f32_32x32x16_bf16 v[224:239], v[204:207], v[44:47], v[224:239]\n"
"v_mov_b32 v244, 161848\n"
"v_mov_b32 v245, 1\n"
"s_mov_b64 exec, 1\n"
"ds_add_u32 v244, v245\n"
"s_mov_b64 exec, -1\n"
"s_and_b32 s61, s7, 1\n"
"s_lshl_b32 s34, s61, 3\n"
"s_mov_b32 s60, 0x3ea52b5c\n"
"s_nop 2\n"
"v_mul_f32 v208, s60, v208\n"
"v_mul_f32 v209, s60, v209\n"
"v_mul_f32 v210, s60, v210\n"
"v_mul_f32 v211, s60, v211\n"
"v_mul_f32 v212, s60, v212\n"
"v_mul_f32 v213, s60, v213\n"
"v_mul_f32 v214, s60, v214\n"
"v_mul_f32 v215, s60, v215\n"
"v_mul_f32 v216, s60, v216\n"
"v_mul_f32 v217, s60, v217\n"
"v_mul_f32 v218, s60, v218\n"
"v_mul_f32 v219, s60, v219\n"
"v_cvt_pk_bf16_f32 v4, v208, v209\n"
"v_cvt_pk_bf16_f32 v5, v210, v211\n"
"v_cvt_pk_bf16_f32 v6, v212, v213\n"
"v_cvt_pk_bf16_f32 v7, v214, v215\n"
"v_cvt_pk_bf16_f32 v8, v216, v217\n"
"v_cvt_pk_bf16_f32 v9, v218, v219\n"
"v_mov_b32 v10, 0\n"
"v_mov_b32 v11, 0\n"
"v_mul_f32 v224, s60, v224\n"
"v_mul_f32 v225, s60, v225\n"
"v_mul_f32 v226, s60, v226\n"
"v_mul_f32 v227, s60, v227\n"
"v_mul_f32 v228, s60, v228\n"
"v_mul_f32 v229, s60, v229\n"
"v_mul_f32 v230, s60, v230\n"
"v_mul_f32 v231, s60, v231\n"
"v_mul_f32 v232, s60, v232\n"
"v_mul_f32 v233, s60, v233\n"
"v_mul_f32 v234, s60, v234\n"
"v_mul_f32 v235, s60, v235\n"
"v_cvt_pk_bf16_f32 v12, v224, v225\n"
"v_cvt_pk_bf16_f32 v13, v226, v227\n"
"v_cvt_pk_bf16_f32 v14, v228, v229\n"
"v_cvt_pk_bf16_f32 v15, v230, v231\n"
"v_cvt_pk_bf16_f32 v16, v232, v233\n"
"v_cvt_pk_bf16_f32 v17, v234, v235\n"
"v_mov_b32 v18, 0\n"
"v_mov_b32 v19, 0\n"
"s_mov_b32 exec_lo, 0\n"
"v_mov_b32 v8, 0\n"
"v_mov_b32 v9, 0\n"
"v_mov_b32 v16, 0\n"
"v_mov_b32 v17, 0\n"
"s_mov_b64 exec, -1\n"
"v_lshlrev_b32 v112, 4, v2\n"
"v_lshl_add_u32 v112, v3, 9, v112\n"
"v_lshlrev_b32 v113, 3, v2\n"
"v_add_u32 v113, 1024, v113\n"
"v_min_u32 v136, 20, v2\n"
"v_cmp_lt_u32 vcc, 27, v2\n"
"v_mov_b32 v137, 19\n"
"s_nop 0\n"
"v_cndmask_b32 v136, v136, v137, vcc\n"
"v_lshlrev_b32 v114, 4, v136\n"
"v_mul_u32_u24 v137, 336, v3\n"
"v_add_u32 v114, v114, v137\n"
"v_add_u32 v114, 1280, v114\n"
"v_and_b32 v130, 3, v1\n"
"v_lshlrev_b32 v130, 2, v130\n"
"v_add_u32 v130, 161792, v130\n"
"s_lshl_b32 s60, s7, 2\n"
"s_add_u32 s60, s60, 161888\n"
"v_mov_b32 v132, s60\n"
"v_mov_b32 v133, 1\n"
"v_mov_b32 v98, 0x3f80\n"
"v_mov_b32 v99, 0\n"
"v_mov_b32 v88, 0\n"
"v_mov_b32 v104, 0\n"
"v_mov_b32 v89, 0\n"
"v_mov_b32 v105, 0\n"
"v_mov_b32 v90, 0\n"
"v_mov_b32 v106, 0\n"
"v_mov_b32 v91, 0\n"
"v_mov_b32 v107, 0\n"
"v_mov_b32 v146, 0x3f80\n"
"v_mov_b32 v147, 0\n"
"v_mfma_f32_32x32x16_bf16 v[20:35], v[88:91], v[88:91], 0\n"
"v_mfma_f32_32x32x16_bf16 v[36:51], v[88:91], v[88:91], 0\n"
"s_lshl_b32 s60, s30, 3\n"
"s_add_u32 s60, s60, s31\n"
"s_mul_i32 s60, s60, 10240\n"
"s_add_u32 s60, s60, 524288\n"
"s_lshl_b32 s64, s7, 10\n"
"s_add_u32 s64, s64, s60\n"
"s_lshr_b32 s65, s7, 1\n"
"s_lshl_b32 s65, s65, 9\n"
"s_add_u32 s65, s65, s34\n"
"s_add_u32 s65, s65, 4096\n"
"s_add_u32 s65, s65, s60\n"
"v_lshlrev_b32 v232, 4, v1\n"
"v_lshlrev_b32 v233, 4, v2\n"
"s_mov_b32 s69, 0\n"
"L_sp8:\n"
"ds_read_b32 v131, v130\n"
"s_waitcnt lgkmcnt(0)\n"
"v_cmp_gt_u32 vcc, 1, v131\n"
"s_cbranch_vccz L_spd8\n"
"s_add_u32 s69, s69, 1\n"
"s_cmp_lt_u32 s69, 100000\n"
"s_cbranch_scc1 L_sp8\n"
"L_spd8:\n"
"ds_read_b128 v[92:95], v112 offset:0\n"
"ds_read_b64 v[96:97], v113 offset:0\n"
"ds_read_b128 v[140:143], v112 offset:2624\n"
"ds_read_b64 v[144:145], v113 offset:2624\n"
"ds_read_b128 v[100:103], v114 offset:0\n"
"ds_read_b128 v[108:111], v114 offset:2624\n"
"s_waitcnt lgkmcnt(2)\n"
"v_mfma_f32_32x32x16_bf16 v[52:67], v[92:95], v[4:7], 0\n"
"v_mfma_f32_32x32x16_bf16 v[52:67], v[96:99], v[8:11], v[52:67]\n"
"v_mfma_f32_32x32x16_bf16 v[148:163], v[140:143], v[4:7], 0\n"
"v_mfma_f32_32x32x16_bf16 v[148:163], v[144:147], v[8:11], v[148:163]\n"
"v_mfma_f32_32x32x16_bf16 v[68:83], v[92:95], v[12:15], 0\n"
"v_mfma_f32_32x32x16_bf16 v[68:83], v[96:99], v[16:19], v[68:83]\n"
"v_mfma_f32_32x32x16_bf16 v[164:179], v[140:143], v[12:15], 0\n"
"v_mfma_f32_32x32x16_bf16 v[164:179], v[144:147], v[16:19], v[164:179]\n"
"s_nop 5\n"
"v_max3_f32 v134, v52, v53, v54\n"
"v_max3_f32 v134, v134, v55, v56\n"
"v_max3_f32 v134, v134, v57, v58\n"
"v_max3_f32 v134, v134, v59, v60\n"
"v_max3_f32 v134, v134, v61, v62\n"
"v_max3_f32 v134, v134, v63, v64\n"
"v_max3_f32 v134, v134, v65, v66\n"
"v_max3_f32 v134, v134, v67, v148\n"
"v_max3_f32 v134, v134, v149, v150\n"
"v_max3_f32 v134, v134, v151, v152\n"
"v_max3_f32 v134, v134, v153, v154\n"
"v_max3_f32 v134, v134, v155, v156\n"
"v_max3_f32 v134, v134, v157, v158\n"
"v_max3_f32 v134, v134, v159, v160\n"
"v_max3_f32 v134, v134, v161, v162\n"
"v_max_f32 v134, v134, v163\n"
"v_mov_b32 v135, v134\n"
"s_nop 1\n"
"v_permlane32_swap_b32 v134, v135\n"
"v_max_f32 v134, v134, v135\n"
"v_sub_f32 v134, 0xc2c80000, v134\n"
"v_cvt_pk_bf16_f32 v134, v134, v88\n"
"s_mov_b32 exec_hi, 0\n"
"v_mov_b32 v10, v134\n"
"s_mov_b64 exec, -1\n"
"v_mfma_f32_32x32x16_bf16 v[52:67], v[92:95], v[4:7], 0\n"
"v_mfma_f32_32x32x16_bf16 v[52:67], v[96:99], v[8:11], v[52:67]\n"
"v_max3_f32 v136, v68, v69, v70\n"
"v_max3_f32 v136, v136, v71, v72\n"
"v_max3_f32 v136, v136, v73, v74\n"
"v_max3_f32 v136, v136, v75, v76\n"
"v_max3_f32 v136, v136, v77, v78\n"
"v_max3_f32 v136, v136, v79, v80\n"
"v_max3_f32 v136, v136, v81, v82\n"
"v_max3_f32 v136, v136, v83, v164\n"
"v_max3_f32 v136, v136, v165, v166\n"
"v_max3_f32 v136, v136, v167, v168\n"
"v_max3_f32 v136, v136, v169, v170\n"
"v_max3_f32 v136, v136, v171, v172\n"
"v_max3_f32 v136, v136, v173, v174\n"
"v_max3_f32 v136, v136, v175, v176\n"
"v_max3_f32 v136, v136, v177, v178\n"
"v_max_f32 v136, v136, v179\n"
"v_mov_b32 v137, v136\n"
"s_nop 1\n"
"v_permlane32_swap_b32 v136, v137\n"
"v_max_f32 v136, v136, v137\n"
"v_sub_f32 v136, 0xc2c80000, v136\n"
"v_cvt_pk_bf16_f32 v136, v136, v88\n"
"s_mov_b32 exec_hi, 0\n"
"v_mov_b32 v18, v136\n"
"s_mov_b64 exec, -1\n"
"s_waitcnt lgkmcnt(0)\n"
"s_mov_b32 s70, 2\n"
"s_mov_b32 s71, 4\n"
"L_attn:\n"
"v_mfma_f32_32x32x16_bf16 v[68:83], v[92:95], v[12:15], 0\n"
"ds_read_b128 v[92:95], v112 offset:2624\n"
"v_exp_f32 v52, v52\n"
"v_exp_f32 v53, v53\n"
"v_exp_f32 v54, v54\n"
"v_mfma_f32_32x32x16_bf16 v[36:51], v[104:107], v[88:91], v[36:51]\n"
"ds_read_b128 v[104:107], v114 offset:672\n"
"v_cvt_pk_bf16_f32 v84, v52, v53\n"
"v_exp_f32 v55, v55\n"
"v_exp_f32 v56, v56\n"
"v_cvt_pk_bf16_f32 v85, v54, v55\n"
"v_exp_f32 v57, v57\n"
"v_exp_f32 v58, v58\n"
"v_cvt_pk_bf16_f32 v86, v56, v57\n"
"v_mfma_f32_32x32x16_bf16 v[68:83], v[96:99], v[16:19], v[68:83]\n"
"ds_read_b64 v[96:97], v113 offset:2624\n"
"v_exp_f32 v59, v59\n"
"v_exp_f32 v60, v60\n"
"v_cvt_pk_bf16_f32 v87, v58, v59\n"
"v_exp_f32 v61, v61\n"
"v_exp_f32 v62, v62\n"
"v_cvt_pk_bf16_f32 v88, v60, v61\n"
"v_mfma_f32_32x32x16_bf16 v[20:35], v[100:103], v[84:87], v[20:35]\n"
"v_exp_f32 v63, v63\n"
"v_exp_f32 v64, v64\n"
"v_cvt_pk_bf16_f32 v89, v62, v63\n"
"v_exp_f32 v65, v65\n"
"v_exp_f32 v66, v66\n"
"v_exp_f32 v67, v67\n"
"s_waitcnt lgkmcnt(0)\n"
"v_cvt_pk_bf16_f32 v90, v64, v65\n"
"v_cvt_pk_bf16_f32 v91, v66, v67\n"
"v_mfma_f32_32x32x16_bf16 v[52:67], v[92:95], v[4:7], 0\n"
"v_exp_f32 v68, v68\n"
"v_exp_f32 v69, v69\n"
"v_exp_f32 v70, v70\n"
"v_mfma_f32_32x32x16_bf16 v[20:35], v[104:107], v[88:91], v[20:35]\n"
"v_cvt_pk_bf16_f32 v84, v68, v69\n"
"v_exp_f32 v71, v71\n"
"v_exp_f32 v72, v72\n"
"v_cvt_pk_bf16_f32 v85, v70, v71\n"
"v_exp_f32 v73, v73\n"
"v_exp_f32 v74, v74\n"
"v_cvt_pk_bf16_f32 v86, v72, v73\n"
"v_mfma_f32_32x32x16_bf16 v[52:67], v[96:99], v[8:11], v[52:67]\n"
"v_exp_f32 v75, v75\n"
"v_exp_f32 v76, v76\n"
"v_cvt_pk_bf16_f32 v87, v74, v75\n"
"v_exp_f32 v77, v77\n"
"v_exp_f32 v78, v78\n"
"v_cvt_pk_bf16_f32 v88, v76, v77\n"
"v_mfma_f32_32x32x16_bf16 v[36:51], v[100:103], v[84:87], v[36:51]\n"
"ds_read_b128 v[100:103], v114 offset:5248\n"
"v_exp_f32 v79, v79\n"
"v_exp_f32 v80, v80\n"
"v_cvt_pk_bf16_f32 v89, v78, v79\n"
"v_exp_f32 v81, v81\n"
"v_exp_f32 v82, v82\n"
"v_exp_f32 v83, v83\n"
"v_cvt_pk_bf16_f32 v90, v80, v81\n"
"v_cvt_pk_bf16_f32 v91, v82, v83\n"
"v_mfma_f32_32x32x16_bf16 v[68:83], v[92:95], v[12:15], 0\n"
"ds_read_b128 v[92:95], v112 offset:5248\n"
"v_exp_f32 v52, v52\n"
"v_exp_f32 v53, v53\n"
"v_exp_f32 v54, v54\n"
"v_mfma_f32_32x32x16_bf16 v[36:51], v[104:107], v[88:91], v[36:51]\n"
"ds_read_b128 v[104:107], v114 offset:3296\n"
"v_cvt_pk_bf16_f32 v84, v52, v53\n"
"v_exp_f32 v55, v55\n"
"v_exp_f32 v56, v56\n"
"v_cvt_pk_bf16_f32 v85, v54, v55\n"
"v_exp_f32 v57, v57\n"
"v_exp_f32 v58, v58\n"
"v_cvt_pk_bf16_f32 v86, v56, v57\n"
"v_mfma_f32_32x32x16_bf16 v[68:83], v[96:99], v[16:19], v[68:83]\n"
"ds_read_b64 v[96:97], v113 offset:5248\n"
"v_exp_f32 v59, v59\n"
"v_exp_f32 v60, v60\n"
"v_cvt_pk_bf16_f32 v87, v58, v59\n"
"v_exp_f32 v61, v61\n"
"v_exp_f32 v62, v62\n"
"v_cvt_pk_bf16_f32 v88, v60, v61\n"
"v_mfma_f32_32x32x16_bf16 v[20:35], v[108:111], v[84:87], v[20:35]\n"
"v_exp_f32 v63, v63\n"
"v_exp_f32 v64, v64\n"
"v_cvt_pk_bf16_f32 v89, v62, v63\n"
"v_exp_f32 v65, v65\n"
"v_exp_f32 v66, v66\n"
"v_exp_f32 v67, v67\n"
"s_waitcnt lgkmcnt(0)\n"
"v_cvt_pk_bf16_f32 v90, v64, v65\n"
"v_cvt_pk_bf16_f32 v91, v66, v67\n"
"v_mfma_f32_32x32x16_bf16 v[52:67], v[92:95], v[4:7], 0\n"
"v_exp_f32 v68, v68\n"
"v_exp_f32 v69, v69\n"
"v_exp_f32 v70, v70\n"
"v_mfma_f32_32x32x16_bf16 v[20:35], v[104:107], v[88:91], v[20:35]\n"
"v_cvt_pk_bf16_f32 v84, v68, v69\n"
"v_exp_f32 v71, v71\n"
"v_exp_f32 v72, v72\n"
"v_cvt_pk_bf16_f32 v85, v70, v71\n"
"v_exp_f32 v73, v73\n"
"v_exp_f32 v74, v74\n"
"v_cvt_pk_bf16_f32 v86, v72, v73\n"
"v_mfma_f32_32x32x16_bf16 v[52:67], v[96:99], v[8:11], v[52:67]\n"
"v_exp_f32 v75, v75\n"
"v_exp_f32 v76, v76\n"
"v_cvt_pk_bf16_f32 v87, v74, v75\n"
"v_exp_f32 v77, v77\n"
"v_exp_f32 v78, v78\n"
"v_cvt_pk_bf16_f32 v88, v76, v77\n"
"v_mfma_f32_32x32x16_bf16 v[36:51], v[108:111], v[84:87], v[36:51]\n"
"ds_read_b128 v[108:111], v114 offset:7872\n"
"v_exp_f32 v79, v79\n"
"v_exp_f32 v80, v80\n"
"v_cvt_pk_bf16_f32 v89, v78, v79\n"
"v_exp_f32 v81, v81\n"
"v_exp_f32 v82, v82\n"
"v_exp_f32 v83, v83\n"
"v_cvt_pk_bf16_f32 v90, v80, v81\n"
"v_cvt_pk_bf16_f32 v91, v82, v83\n"
"v_mfma_f32_32x32x16_bf16 v[68:83], v[92:95], v[12:15], 0\n"
"ds_read_b128 v[92:95], v112 offset:7872\n"
"v_exp_f32 v52, v52\n"
"v_exp_f32 v53, v53\n"
"v_exp_f32 v54, v54\n"
"v_mfma_f32_32x32x16_bf16 v[36:51], v[104:107], v[88:91], v[36:51]\n"
"ds_read_b128 v[104:107], v114 offset:5920\n"
"v_cvt_pk_bf16_f32 v84, v52, v53\n"
"v_exp_f32 v55, v55\n"
"v_exp_f32 v56, v56\n"
"v_cvt_pk_bf16_f32 v85, v54, v55\n"
"v_exp_f32 v57, v57\n"
"v_exp_f32 v58, v58\n"
"v_cvt_pk_bf16_f32 v86, v56, v57\n"
"v_mfma_f32_32x32x16_bf16 v[68:83], v[96:99], v[16:19], v[68:83]\n"
"ds_read_b64 v[96:97], v113 offset:7872\n"
"v_exp_f32 v59, v59\n"
"v_exp_f32 v60, v60\n"
"v_cvt_pk_bf16_f32 v87, v58, v59\n"
"v_exp_f32 v61, v61\n"
"v_exp_f32 v62, v62\n"
"v_cvt_pk_bf16_f32 v88, v60, v61\n"
"v_mfma_f32_32x32x16_bf16 v[20:35], v[100:103], v[84:87], v[20:35]\n"
"v_exp_f32 v63, v63\n"
"v_exp_f32 v64, v64\n"
"v_cvt_pk_bf16_f32 v89, v62, v63\n"
"v_exp_f32 v65, v65\n"
"v_exp_f32 v66, v66\n"
"v_exp_f32 v67, v67\n"
"s_waitcnt lgkmcnt(0)\n"
"v_cvt_pk_bf16_f32 v90, v64, v65\n"
"v_cvt_pk_bf16_f32 v91, v66, v67\n"
"v_mfma_f32_32x32x16_bf16 v[52:67], v[92:95], v[4:7], 0\n"
"ds_read_b32 v131, v130\n"
"v_exp_f32 v68, v68\n"
"v_exp_f32 v69, v69\n"
"v_exp_f32 v70, v70\n"
"v_mfma_f32_32x32x16_bf16 v[20:35], v[104:107], v[88:91], v[20:35]\n"
"v_cvt_pk_bf16_f32 v84, v68, v69\n"
"v_exp_f32 v71, v71\n"
"v_exp_f32 v72, v72\n"
"v_cvt_pk_bf16_f32 v85, v70, v71\n"
"v_exp_f32 v73, v73\n"
"v_exp_f32 v74, v74\n"
"v_cvt_pk_bf16_f32 v86, v72, v73\n"
"v_mfma_f32_32x32x16_bf16 v[52:67], v[96:99], v[8:11], v[52:67]\n"
"v_exp_f32 v75, v75\n"
"v_exp_f32 v76, v76\n"
"v_cvt_pk_bf16_f32 v87, v74, v75\n"
"v_exp_f32 v77, v77\n"
"v_exp_f32 v78, v78\n"
"v_cvt_pk_bf16_f32 v88, v76, v77\n"
"v_mfma_f32_32x32x16_bf16 v[36:51], v[100:103], v[84:87], v[36:51]\n"
"s_waitcnt lgkmcnt(0)\n"
"v_cmp_gt_u32 vcc, s70, v131\n"
"s_cbranch_vccnz L_rs9\n"
"L_rdy9:\n"
"s_add_u32 s70, s70, 1\n"
"ds_read_b128 v[100:103], v114 offset:10496\n"
"v_exp_f32 v79, v79\n"
"v_exp_f32 v80, v80\n"
"v_cvt_pk_bf16_f32 v89, v78, v79\n"
"v_exp_f32 v81, v81\n"
"v_exp_f32 v82, v82\n"
"v_exp_f32 v83, v83\n"
"v_cvt_pk_bf16_f32 v90, v80, v81\n"
"v_cvt_pk_bf16_f32 v91, v82, v83\n"
"v_mfma_f32_32x32x16_bf16 v[68:83], v[92:95], v[12:15], 0\n"
"ds_read_b128 v[92:95], v112 offset:10496\n"
"v_exp_f32 v52, v52\n"
"v_exp_f32 v53, v53\n"
"v_exp_f32 v54, v54\n"
"v_mfma_f32_32x32x16_bf16 v[36:51], v[104:107], v[88:91], v[36:51]\n"
"ds_read_b128 v[104:107], v114 offset:8544\n"
"v_cvt_pk_bf16_f32 v84, v52, v53\n"
"v_exp_f32 v55, v55\n"
"v_exp_f32 v56, v56\n"
"v_cvt_pk_bf16_f32 v85, v54, v55\n"
"v_exp_f32 v57, v57\n"
"v_exp_f32 v58, v58\n"
"v_cvt_pk_bf16_f32 v86, v56, v57\n"
"v_mfma_f32_32x32x16_bf16 v[68:83], v[96:99], v[16:19], v[68:83]\n"
"ds_read_b64 v[96:97], v113 offset:10496\n"
"v_exp_f32 v59, v59\n"
"v_exp_f32 v60, v60\n"
"v_cvt_pk_bf16_f32 v87, v58, v59\n"
"v_exp_f32 v61, v61\n"
"v_exp_f32 v62, v62\n"
"v_cvt_pk_bf16_f32 v88, v60, v61\n"
"v_mfma_f32_32x32x16_bf16 v[20:35], v[108:111], v[84:87], v[20:35]\n"
"v_exp_f32 v63, v63\n"
"v_exp_f32 v64, v64\n"
"v_cvt_pk_bf16_f32 v89, v62, v63\n"
"v_exp_f32 v65, v65\n"
"v_exp_f32 v66, v66\n"
"v_exp_f32 v67, v67\n"
"s_waitcnt lgkmcnt(0)\n"
"v_cvt_pk_bf16_f32 v90, v64, v65\n"
"v_cvt_pk_bf16_f32 v91, v66, v67\n"
"v_mfma_f32_32x32x16_bf16 v[52:67], v[92:95], v[4:7], 0\n"
"s_mov_b64 exec, 1\n"
"ds_add_u32 v132, v133\n"
"s_mov_b64 exec, -1\n"
"v_exp_f32 v68, v68\n"
"v_exp_f32 v69, v69\n"
"v_exp_f32 v70, v70\n"
"v_mfma_f32_32x32x16_bf16 v[20:35], v[104:107], v[88:91], v[20:35]\n"
"v_cvt_pk_bf16_f32 v84, v68, v69\n"
"v_exp_f32 v71, v71\n"
"v_exp_f32 v72, v72\n"
"v_cvt_pk_bf16_f32 v85, v70, v71\n"
"v_exp_f32 v73, v73\n"
"v_exp_f32 v74, v74\n"
"v_cvt_pk_bf16_f32 v86, v72, v73\n"
"v_mfma_f32_32x32x16_bf16 v[52:67], v[96:99], v[8:11], v[52:67]\n"
"v_exp_f32 v75, v75\n"
"v_exp_f32 v76, v76\n"
"v_cvt_pk_bf16_f32 v87, v74, v75\n"
"v_exp_f32 v77, v77\n"
"v_exp_f32 v78, v78\n"
"v_cvt_pk_bf16_f32 v88, v76, v77\n"
"v_mfma_f32_32x32x16_bf16 v[36:51], v[108:111], v[84:87], v[36:51]\n"
"ds_read_b128 v[108:111], v114 offset:13120\n"
"v_exp_f32 v79, v79\n"
"v_exp_f32 v80, v80\n"
"v_cvt_pk_bf16_f32 v89, v78, v79\n"
"v_exp_f32 v81, v81\n"
"v_exp_f32 v82, v82\n"
"v_exp_f32 v83, v83\n"
"v_cvt_pk_bf16_f32 v90, v80, v81\n"
"v_cvt_pk_bf16_f32 v91, v82, v83\n"
"v_mfma_f32_32x32x16_bf16 v[68:83], v[92:95], v[12:15], 0\n"
"ds_read_b128 v[92:95], v112 offset:13120\n"
"v_exp_f32 v52, v52\n"
"v_exp_f32 v53, v53\n"
"v_exp_f32 v54, v54\n"
"v_mfma_f32_32x32x16_bf16 v[36:51], v[104:107], v[88:91], v[36:51]\n"
"ds_read_b128 v[104:107], v114 offset:11168\n"
"v_cvt_pk_bf16_f32 v84, v52, v53\n"
"v_exp_f32 v55, v55\n"
"v_exp_f32 v56, v56\n"
"v_cvt_pk_bf16_f32 v85, v54, v55\n"
"v_exp_f32 v57, v57\n"
"v_exp_f32 v58, v58\n"
"v_cvt_pk_bf16_f32 v86, v56, v57\n"
"v_mfma_f32_32x32x16_bf16 v[68:83], v[96:99], v[16:19], v[68:83]\n"
"ds_read_b64 v[96:97], v113 offset:13120\n"
"v_exp_f32 v59, v59\n"
"v_exp_f32 v60, v60\n"
"v_cvt_pk_bf16_f32 v87, v58, v59\n"
"v_exp_f32 v61, v61\n"
"v_exp_f32 v62, v62\n"
"v_cvt_pk_bf16_f32 v88, v60, v61\n"
"v_mfma_f32_32x32x16_bf16 v[20:35], v[100:103], v[84:87], v[20:35]\n"
"v_exp_f32 v63, v63\n"
"v_exp_f32 v64, v64\n"
"v_cvt_pk_bf16_f32 v89, v62, v63\n"
"v_exp_f32 v65, v65\n"
"v_exp_f32 v66, v66\n"
"v_exp_f32 v67, v67\n"
"s_waitcnt lgkmcnt(0)\n"
"v_cvt_pk_bf16_f32 v90, v64, v65\n"
"v_cvt_pk_bf16_f32 v91, v66, v67\n"
"v_mfma_f32_32x32x16_bf16 v[52:67], v[92:95], v[4:7], 0\n"
"v_exp_f32 v68, v68\n"
"v_exp_f32 v69, v69\n"
"v_exp_f32 v70, v70\n"
"v_mfma_f32_32x32x16_bf16 v[20:35], v[104:107], v[88:91], v[20:35]\n"
"v_cvt_pk_bf16_f32 v84, v68, v69\n"
"v_exp_f32 v71, v71\n"
"v_exp_f32 v72, v72\n"
"v_cvt_pk_bf16_f32 v85, v70, v71\n"
"v_exp_f32 v73, v73\n"
"v_exp_f32 v74, v74\n"
"v_cvt_pk_bf16_f32 v86, v72, v73\n"
"v_mfma_f32_32x32x16_bf16 v[52:67], v[96:99], v[8:11], v[52:67]\n"
"v_exp_f32 v75, v75\n"
"v_exp_f32 v76, v76\n"
"v_cvt_pk_bf16_f32 v87, v74, v75\n"
"v_exp_f32 v77, v77\n"
"v_exp_f32 v78, v78\n"
"v_cvt_pk_bf16_f32 v88, v76, v77\n"
"v_mfma_f32_32x32x16_bf16 v[36:51], v[100:103], v[84:87], v[36:51]\n"
"ds_read_b128 v[100:103], v114 offset:15744\n"
"v_exp_f32 v79, v79\n"
"v_exp_f32 v80, v80\n"
"v_cvt_pk_bf16_f32 v89, v78, v79\n"
"v_exp_f32 v81, v81\n"
"v_exp_f32 v82, v82\n"
"v_exp_f32 v83, v83\n"
"v_cvt_pk_bf16_f32 v90, v80, v81\n"
"v_cvt_pk_bf16_f32 v91, v82, v83\n"
"v_mfma_f32_32x32x16_bf16 v[68:83], v[92:95], v[12:15], 0\n"
"ds_read_b128 v[92:95], v112 offset:15744\n"
"v_exp_f32 v52, v52\n"
"v_exp_f32 v53, v53\n"
"v_exp_f32 v54, v54\n"
"v_mfma_f32_32x32x16_bf16 v[36:51], v[104:107], v[88:91], v[36:51]\n"
"ds_read_b128 v[104:107], v114 offset:13792\n"
"v_cvt_pk_bf16_f32 v84, v52, v53\n"
"v_exp_f32 v55, v55\n"
"v_exp_f32 v56, v56\n"
"v_cvt_pk_bf16_f32 v85, v54, v55\n"
"v_exp_f32 v57, v57\n"
"v_exp_f32 v58, v58\n"
"v_cvt_pk_bf16_f32 v86, v56, v57\n"
"v_mfma_f32_32x32x16_bf16 v[68:83], v[96:99], v[16:19], v[68:83]\n"
"ds_read_b64 v[96:97], v113 offset:15744\n"
"v_exp_f32 v59, v59\n"
"v_exp_f32 v60, v60\n"
"v_cvt_pk_bf16_f32 v87, v58, v59\n"
"v_exp_f32 v61, v61\n"
"v_exp_f32 v62, v62\n"
"v_cvt_pk_bf16_f32 v88, v60, v61\n"
"v_mfma_f32_32x32x16_bf16 v[20:35], v[108:111], v[84:87], v[20:35]\n"
"v_exp_f32 v63, v63\n"
"v_exp_f32 v64, v64\n"
"v_cvt_pk_bf16_f32 v89, v62, v63\n"
"v_exp_f32 v65, v65\n"
"v_exp_f32 v66, v66\n"
"v_exp_f32 v67, v67\n"
"s_waitcnt lgkmcnt(0)\n"
"v_cvt_pk_bf16_f32 v90, v64, v65\n"
"v_cvt_pk_bf16_f32 v91, v66, v67\n"
"v_mfma_f32_32x32x16_bf16 v[52:67], v[92:95], v[4:7], 0\n"
"v_exp_f32 v68, v68\n"
"v_exp_f32 v69, v69\n"
"v_exp_f32 v70, v70\n"
"v_mfma_f32_32x32x16_bf16 v[20:35], v[104:107], v[88:91], v[20:35]\n"
"v_cvt_pk_bf16_f32 v84, v68, v69\n"
"v_exp_f32 v71, v71\n"
"v_exp_f32 v72, v72\n"
"v_cvt_pk_bf16_f32 v85, v70, v71\n"
"v_exp_f32 v73, v73\n"
"v_exp_f32 v74, v74\n"
"v_cvt_pk_bf16_f32 v86, v72, v73\n"
"v_mfma_f32_32x32x16_bf16 v[52:67], v[96:99], v[8:11], v[52:67]\n"
"v_exp_f32 v75, v75\n"
"v_exp_f32 v76, v76\n"
"v_cvt_pk_bf16_f32 v87, v74, v75\n"
"v_exp_f32 v77, v77\n"
"v_exp_f32 v78, v78\n"
"v_cvt_pk_bf16_f32 v88, v76, v77\n"
"v_mfma_f32_32x32x16_bf16 v[36:51], v[108:111], v[84:87], v[36:51]\n"
"ds_read_b128 v[108:111], v114 offset:18368\n"
"v_exp_f32 v79, v79\n"
"v_exp_f32 v80, v80\n"
"v_cvt_pk_bf16_f32 v89, v78, v79\n"
"v_exp_f32 v81, v81\n"
"v_exp_f32 v82, v82\n"
"v_exp_f32 v83, v83\n"
"v_cvt_pk_bf16_f32 v90, v80, v81\n"
"v_cvt_pk_bf16_f32 v91, v82, v83\n"
"v_mfma_f32_32x32x16_bf16 v[68:83], v[92:95], v[12:15], 0\n"
"ds_read_b128 v[92:95], v112 offset:18368\n"
"v_exp_f32 v52, v52\n"
"v_exp_f32 v53, v53\n"
"v_exp_f32 v54, v54\n"
"v_mfma_f32_32x32x16_bf16 v[36:51], v[104:107], v[88:91], v[36:51]\n"
"ds_read_b128 v[104:107], v114 offset:16416\n"
"v_cvt_pk_bf16_f32 v84, v52, v53\n"
"v_exp_f32 v55, v55\n"
"v_exp_f32 v56, v56\n"
"v_cvt_pk_bf16_f32 v85, v54, v55\n"
"v_exp_f32 v57, v57\n"
"v_exp_f32 v58, v58\n"
"v_cvt_pk_bf16_f32 v86, v56, v57\n"
"v_mfma_f32_32x32x16_bf16 v[68:83], v[96:99], v[16:19], v[68:83]\n"
"ds_read_b64 v[96:97], v113 offset:18368\n"
"v_exp_f32 v59, v59\n"
"v_exp_f32 v60, v60\n"
"v_cvt_pk_bf16_f32 v87, v58, v59\n"
"v_exp_f32 v61, v61\n"
"v_exp_f32 v62, v62\n"
"v_cvt_pk_bf16_f32 v88, v60, v61\n"
"v_mfma_f32_32x32x16_bf16 v[20:35], v[100:103], v[84:87], v[20:35]\n"
"v_exp_f32 v63, v63\n"
"v_exp_f32 v64, v64\n"
"v_cvt_pk_bf16_f32 v89, v62, v63\n"
"v_exp_f32 v65, v65\n"
"v_exp_f32 v66, v66\n"
"v_exp_f32 v67, v67\n"
"s_waitcnt lgkmcnt(0)\n"
"v_cvt_pk_bf16_f32 v90, v64, v65\n"
"v_cvt_pk_bf16_f32 v91, v66, v67\n"
"v_mfma_f32_32x32x16_bf16 v[52:67], v[92:95], v[4:7], 0\n"
"ds_read_b32 v131, v130\n"
"v_exp_f32 v68, v68\n"
"v_exp_f32 v69, v69\n"
"v_exp_f32 v70, v70\n"
"v_mfma_f32_32x32x16_bf16 v[20:35], v[104:107], v[88:91], v[20:35]\n"
"v_cvt_pk_bf16_f32 v84, v68, v69\n"
"v_exp_f32 v71, v71\n"
"v_exp_f32 v72, v72\n"
"v_cvt_pk_bf16_f32 v85, v70, v71\n"
"v_exp_f32 v73, v73\n"
"v_exp_f32 v74, v74\n"
"v_cvt_pk_bf16_f32 v86, v72, v73\n"
"v_mfma_f32_32x32x16_bf16 v[52:67], v[96:99], v[8:11], v[52:67]\n"
"v_exp_f32 v75, v75\n"
"v_exp_f32 v76, v76\n"
"v_cvt_pk_bf16_f32 v87, v74, v75\n"
"v_exp_f32 v77, v77\n"
"v_exp_f32 v78, v78\n"
"v_cvt_pk_bf16_f32 v88, v76, v77\n"
"v_mfma_f32_32x32x16_bf16 v[36:51], v[100:103], v[84:87], v[36:51]\n"
"s_waitcnt lgkmcnt(0)\n"
"v_cmp_gt_u32 vcc, s70, v131\n"
"s_cbranch_vccnz L_rs10\n"
"L_rdy10:\n"
"s_add_u32 s70, s70, 1\n"
"ds_read_b128 v[100:103], v114 offset:20992\n"
"v_exp_f32 v79, v79\n"
"v_exp_f32 v80, v80\n"
"v_cvt_pk_bf16_f32 v89, v78, v79\n"
"v_exp_f32 v81, v81\n"
"v_exp_f32 v82, v82\n"
"v_exp_f32 v83, v83\n"
"v_cvt_pk_bf16_f32 v90, v80, v81\n"
"v_cvt_pk_bf16_f32 v91, v82, v83\n"
"v_mfma_f32_32x32x16_bf16 v[68:83], v[92:95], v[12:15], 0\n"
"ds_read_b128 v[92:95], v112 offset:20992\n"
"v_exp_f32 v52, v52\n"
"v_exp_f32 v53, v53\n"
"v_exp_f32 v54, v54\n"
"v_mfma_f32_32x32x16_bf16 v[36:51], v[104:107], v[88:91], v[36:51]\n"
"ds_read_b128 v[104:107], v114 offset:19040\n"
"v_cvt_pk_bf16_f32 v84, v52, v53\n"
"v_exp_f32 v55, v55\n"
"v_exp_f32 v56, v56\n"
"v_cvt_pk_bf16_f32 v85, v54, v55\n"
"v_exp_f32 v57, v57\n"
"v_exp_f32 v58, v58\n"
"v_cvt_pk_bf16_f32 v86, v56, v57\n"
"v_mfma_f32_32x32x16_bf16 v[68:83], v[96:99], v[16:19], v[68:83]\n"
"ds_read_b64 v[96:97], v113 offset:20992\n"
"v_exp_f32 v59, v59\n"
"v_exp_f32 v60, v60\n"
"v_cvt_pk_bf16_f32 v87, v58, v59\n"
"v_exp_f32 v61, v61\n"
"v_exp_f32 v62, v62\n"
"v_cvt_pk_bf16_f32 v88, v60, v61\n"
"v_mfma_f32_32x32x16_bf16 v[20:35], v[108:111], v[84:87], v[20:35]\n"
"v_exp_f32 v63, v63\n"
"v_exp_f32 v64, v64\n"
"v_cvt_pk_bf16_f32 v89, v62, v63\n"
"v_exp_f32 v65, v65\n"
"v_exp_f32 v66, v66\n"
"v_exp_f32 v67, v67\n"
"s_waitcnt lgkmcnt(0)\n"
"v_cvt_pk_bf16_f32 v90, v64, v65\n"
"v_cvt_pk_bf16_f32 v91, v66, v67\n"
"v_mfma_f32_32x32x16_bf16 v[52:67], v[92:95], v[4:7], 0\n"
"s_mov_b64 exec, 1\n"
"ds_add_u32 v132, v133\n"
"s_mov_b64 exec, -1\n"
"v_exp_f32 v68, v68\n"
"v_exp_f32 v69, v69\n"
"v_exp_f32 v70, v70\n"
"v_mfma_f32_32x32x16_bf16 v[20:35], v[104:107], v[88:91], v[20:35]\n"
"v_cvt_pk_bf16_f32 v84, v68, v69\n"
"v_exp_f32 v71, v71\n"
"v_exp_f32 v72, v72\n"
"v_cvt_pk_bf16_f32 v85, v70, v71\n"
"v_exp_f32 v73, v73\n"
"v_exp_f32 v74, v74\n"
"v_cvt_pk_bf16_f32 v86, v72, v73\n"
"v_mfma_f32_32x32x16_bf16 v[52:67], v[96:99], v[8:11], v[52:67]\n"
"v_exp_f32 v75, v75\n"
"v_exp_f32 v76, v76\n"
"v_cvt_pk_bf16_f32 v87, v74, v75\n"
"v_exp_f32 v77, v77\n"
"v_exp_f32 v78, v78\n"
"v_cvt_pk_bf16_f32 v88, v76, v77\n"
"v_mfma_f32_32x32x16_bf16 v[36:51], v[108:111], v[84:87], v[36:51]\n"
"ds_read_b128 v[108:111], v114 offset:23616\n"
"v_exp_f32 v79, v79\n"
"v_exp_f32 v80, v80\n"
"v_cvt_pk_bf16_f32 v89, v78, v79\n"
"v_exp_f32 v81, v81\n"
"v_exp_f32 v82, v82\n"
"v_exp_f32 v83, v83\n"
"v_cvt_pk_bf16_f32 v90, v80, v81\n"
"v_cvt_pk_bf16_f32 v91, v82, v83\n"
"v_mfma_f32_32x32x16_bf16 v[68:83], v[92:95], v[12:15], 0\n"
"ds_read_b128 v[92:95], v112 offset:23616\n"
"v_exp_f32 v52, v52\n"
"v_exp_f32 v53, v53\n"
"v_exp_f32 v54, v54\n"
"v_mfma_f32_32x32x16_bf16 v[36:51], v[104:107], v[88:91], v[36:51]\n"
"ds_read_b128 v[104:107], v114 offset:21664\n"
"v_cvt_pk_bf16_f32 v84, v52, v53\n"
"v_exp_f32 v55, v55\n"
"v_exp_f32 v56, v56\n"
"v_cvt_pk_bf16_f32 v85, v54, v55\n"
"v_exp_f32 v57, v57\n"
"v_exp_f32 v58, v58\n"
"v_cvt_pk_bf16_f32 v86, v56, v57\n"
"v_mfma_f32_32x32x16_bf16 v[68:83], v[96:99], v[16:19], v[68:83]\n"
"ds_read_b64 v[96:97], v113 offset:23616\n"
"v_exp_f32 v59, v59\n"
"v_exp_f32 v60, v60\n"
"v_cvt_pk_bf16_f32 v87, v58, v59\n"
"v_exp_f32 v61, v61\n"
"v_exp_f32 v62, v62\n"
"v_cvt_pk_bf16_f32 v88, v60, v61\n"
"v_mfma_f32_32x32x16_bf16 v[20:35], v[100:103], v[84:87], v[20:35]\n"
"v_exp_f32 v63, v63\n"
"v_exp_f32 v64, v64\n"
"v_cvt_pk_bf16_f32 v89, v62, v63\n"
"v_exp_f32 v65, v65\n"
"v_exp_f32 v66, v66\n"
"v_exp_f32 v67, v67\n"
"s_waitcnt lgkmcnt(0)\n"
"v_cvt_pk_bf16_f32 v90, v64, v65\n"
"v_cvt_pk_bf16_f32 v91, v66, v67\n"
"v_mfma_f32_32x32x16_bf16 v[52:67], v[92:95], v[4:7], 0\n"
"v_exp_f32 v68, v68\n"
"v_exp_f32 v69, v69\n"
"v_exp_f32 v70, v70\n"
"v_mfma_f32_32x32x16_bf16 v[20:35], v[104:107], v[88:91], v[20:35]\n"
"v_cvt_pk_bf16_f32 v84, v68, v69\n"
"v_exp_f32 v71, v71\n"
"v_exp_f32 v72, v72\n"
"v_cvt_pk_bf16_f32 v85, v70, v71\n"
"v_exp_f32 v73, v73\n"
"v_exp_f32 v74, v74\n"
"v_cvt_pk_bf16_f32 v86, v72, v73\n"
"v_mfma_f32_32x32x16_bf16 v[52:67], v[96:99], v[8:11], v[52:67]\n"
"v_exp_f32 v75, v75\n"
"v_exp_f32 v76, v76\n"
"v_cvt_pk_bf16_f32 v87, v74, v75\n"
"v_exp_f32 v77, v77\n"
"v_exp_f32 v78, v78\n"
"v_cvt_pk_bf16_f32 v88, v76, v77\n"
"v_mfma_f32_32x32x16_bf16 v[36:51], v[100:103], v[84:87], v[36:51]\n"
"ds_read_b128 v[100:103], v114 offset:26240\n"
"v_exp_f32 v79, v79\n"
"v_exp_f32 v80, v80\n"
"v_cvt_pk_bf16_f32 v89, v78, v79\n"
"v_exp_f32 v81, v81\n"
"v_exp_f32 v82, v82\n"
"v_exp_f32 v83, v83\n"
"v_cvt_pk_bf16_f32 v90, v80, v81\n"
"v_cvt_pk_bf16_f32 v91, v82, v83\n"
"v_mfma_f32_32x32x16_bf16 v[68:83], v[92:95], v[12:15], 0\n"
"ds_read_b128 v[92:95], v112 offset:26240\n"
"v_exp_f32 v52, v52\n"
"v_exp_f32 v53, v53\n"
"v_exp_f32 v54, v54\n"
"v_mfma_f32_32x32x16_bf16 v[36:51], v[104:107], v[88:91], v[36:51]\n"
"ds_read_b128 v[104:107], v114 offset:24288\n"
"v_cvt_pk_bf16_f32 v84, v52, v53\n"
"v_exp_f32 v55, v55\n"
"v_exp_f32 v56, v56\n"
"v_cvt_pk_bf16_f32 v85, v54, v55\n"
"v_exp_f32 v57, v57\n"
"v_exp_f32 v58, v58\n"
"v_cvt_pk_bf16_f32 v86, v56, v57\n"
"v_mfma_f32_32x32x16_bf16 v[68:83], v[96:99], v[16:19], v[68:83]\n"
"ds_read_b64 v[96:97], v113 offset:26240\n"
"v_exp_f32 v59, v59\n"
"v_exp_f32 v60, v60\n"
"v_cvt_pk_bf16_f32 v87, v58, v59\n"
"v_exp_f32 v61, v61\n"
"v_exp_f32 v62, v62\n"
"v_cvt_pk_bf16_f32 v88, v60, v61\n"
"v_mfma_f32_32x32x16_bf16 v[20:35], v[108:111], v[84:87], v[20:35]\n"
"v_exp_f32 v63, v63\n"
"v_exp_f32 v64, v64\n"
"v_cvt_pk_bf16_f32 v89, v62, v63\n"
"v_exp_f32 v65, v65\n"
"v_exp_f32 v66, v66\n"
"v_exp_f32 v67, v67\n"
"s_waitcnt lgkmcnt(0)\n"
"v_cvt_pk_bf16_f32 v90, v64, v65\n"
"v_cvt_pk_bf16_f32 v91, v66, v67\n"
"v_mfma_f32_32x32x16_bf16 v[52:67], v[92:95], v[4:7], 0\n"
"v_exp_f32 v68, v68\n"
"v_exp_f32 v69, v69\n"
"v_exp_f32 v70, v70\n"
"v_mfma_f32_32x32x16_bf16 v[20:35], v[104:107], v[88:91], v[20:35]\n"
"v_cvt_pk_bf16_f32 v84, v68, v69\n"
"v_exp_f32 v71, v71\n"
"v_exp_f32 v72, v72\n"
"v_cvt_pk_bf16_f32 v85, v70, v71\n"
"v_exp_f32 v73, v73\n"
"v_exp_f32 v74, v74\n"
"v_cvt_pk_bf16_f32 v86, v72, v73\n"
"v_mfma_f32_32x32x16_bf16 v[52:67], v[96:99], v[8:11], v[52:67]\n"
"v_exp_f32 v75, v75\n"
"v_exp_f32 v76, v76\n"
"v_cvt_pk_bf16_f32 v87, v74, v75\n"
"v_exp_f32 v77, v77\n"
"v_exp_f32 v78, v78\n"
"v_cvt_pk_bf16_f32 v88, v76, v77\n"
"v_mfma_f32_32x32x16_bf16 v[36:51], v[108:111], v[84:87], v[36:51]\n"
"ds_read_b128 v[108:111], v114 offset:28864\n"
"v_exp_f32 v79, v79\n"
"v_exp_f32 v80, v80\n"
"v_cvt_pk_bf16_f32 v89, v78, v79\n"
"v_exp_f32 v81, v81\n"
"v_exp_f32 v82, v82\n"
"v_exp_f32 v83, v83\n"
"v_cvt_pk_bf16_f32 v90, v80, v81\n"
"v_cvt_pk_bf16_f32 v91, v82, v83\n"
"v_mfma_f32_32x32x16_bf16 v[68:83], v[92:95], v[12:15], 0\n"
"ds_read_b128 v[92:95], v112 offset:28864\n"
"v_exp_f32 v52, v52\n"
"v_exp_f32 v53, v53\n"
"v_exp_f32 v54, v54\n"
"v_mfma_f32_32x32x16_bf16 v[36:51], v[104:107], v[88:91], v[36:51]\n"
"ds_read_b128 v[104:107], v114 offset:26912\n"
"v_cvt_pk_bf16_f32 v84, v52, v53\n"
"v_exp_f32 v55, v55\n"
"v_exp_f32 v56, v56\n"
"v_cvt_pk_bf16_f32 v85, v54, v55\n"
"v_exp_f32 v57, v57\n"
"v_exp_f32 v58, v58\n"
"v_cvt_pk_bf16_f32 v86, v56, v57\n"
"v_mfma_f32_32x32x16_bf16 v[68:83], v[96:99], v[16:19], v[68:83]\n"
"ds_read_b64 v[96:97], v113 offset:28864\n"
"v_exp_f32 v59, v59\n"
"v_exp_f32 v60, v60\n"
"v_cvt_pk_bf16_f32 v87, v58, v59\n"
"v_exp_f32 v61, v61\n"
"v_exp_f32 v62, v62\n"
"v_cvt_pk_bf16_f32 v88, v60, v61\n"
"v_mfma_f32_32x32x16_bf16 v[20:35], v[100:103], v[84:87], v[20:35]\n"
"v_exp_f32 v63, v63\n"
"v_exp_f32 v64, v64\n"
"v_cvt_pk_bf16_f32 v89, v62, v63\n"
"v_exp_f32 v65, v65\n"
"v_exp_f32 v66, v66\n"
"v_exp_f32 v67, v67\n"
"s_waitcnt lgkmcnt(0)\n"
"v_cvt_pk_bf16_f32 v90, v64, v65\n"
"v_cvt_pk_bf16_f32 v91, v66, v67\n"
"v_mfma_f32_32x32x16_bf16 v[52:67], v[92:95], v[4:7], 0\n"
"ds_read_b32 v131, v130\n"
"v_exp_f32 v68, v68\n"
"v_exp_f32 v69, v69\n"
"v_exp_f32 v70, v70\n"
"v_mfma_f32_32x32x16_bf16 v[20:35], v[104:107], v[88:91], v[20:35]\n"
"v_cvt_pk_bf16_f32 v84, v68, v69\n"
"v_exp_f32 v71, v71\n"
"v_exp_f32 v72, v72\n"
"v_cvt_pk_bf16_f32 v85, v70, v71\n"
"v_exp_f32 v73, v73\n"
"v_exp_f32 v74, v74\n"
"v_cvt_pk_bf16_f32 v86, v72, v73\n"
"v_mfma_f32_32x32x16_bf16 v[52:67], v[96:99], v[8:11], v[52:67]\n"
"v_exp_f32 v75, v75\n"
"v_exp_f32 v76, v76\n"
"v_cvt_pk_bf16_f32 v87, v74, v75\n"
"v_exp_f32 v77, v77\n"
"v_exp_f32 v78, v78\n"
"v_cvt_pk_bf16_f32 v88, v76, v77\n"
"v_mfma_f32_32x32x16_bf16 v[36:51], v[100:103], v[84:87], v[36:51]\n"
"s_waitcnt lgkmcnt(0)\n"
"v_cmp_gt_u32 vcc, s70, v131\n"
"s_cbranch_vccnz L_rs11\n"
"L_rdy11:\n"
"s_add_u32 s70, s70, 1\n"
"ds_read_b128 v[100:103], v114 offset:31488\n"
"v_exp_f32 v79, v79\n"
"v_exp_f32 v80, v80\n"
"v_cvt_pk_bf16_f32 v89, v78, v79\n"
"v_exp_f32 v81, v81\n"
"v_exp_f32 v82, v82\n"
"v_exp_f32 v83, v83\n"
"v_cvt_pk_bf16_f32 v90, v80, v81\n"
"v_cvt_pk_bf16_f32 v91, v82, v83\n"
"v_mfma_f32_32x32x16_bf16 v[68:83], v[92:95], v[12:15], 0\n"
"ds_read_b128 v[92:95], v112 offset:31488\n"
"v_exp_f32 v52, v52\n"
"v_exp_f32 v53, v53\n"
"v_exp_f32 v54, v54\n"
"v_mfma_f32_32x32x16_bf16 v[36:51], v[104:107], v[88:91], v[36:51]\n"
"ds_read_b128 v[104:107], v114 offset:29536\n"
"v_cvt_pk_bf16_f32 v84, v52, v53\n"
"v_exp_f32 v55, v55\n"
"v_exp_f32 v56, v56\n"
"v_cvt_pk_bf16_f32 v85, v54, v55\n"
"v_exp_f32 v57, v57\n"
"v_exp_f32 v58, v58\n"
"v_cvt_pk_bf16_f32 v86, v56, v57\n"
"v_mfma_f32_32x32x16_bf16 v[68:83], v[96:99], v[16:19], v[68:83]\n"
"ds_read_b64 v[96:97], v113 offset:31488\n"
"v_exp_f32 v59, v59\n"
"v_exp_f32 v60, v60\n"
"v_cvt_pk_bf16_f32 v87, v58, v59\n"
"v_exp_f32 v61, v61\n"
"v_exp_f32 v62, v62\n"
"v_cvt_pk_bf16_f32 v88, v60, v61\n"
"v_mfma_f32_32x32x16_bf16 v[20:35], v[108:111], v[84:87], v[20:35]\n"
"v_exp_f32 v63, v63\n"
"v_exp_f32 v64, v64\n"
"v_cvt_pk_bf16_f32 v89, v62, v63\n"
"v_exp_f32 v65, v65\n"
"v_exp_f32 v66, v66\n"
"v_exp_f32 v67, v67\n"
"s_waitcnt lgkmcnt(0)\n"
"v_cvt_pk_bf16_f32 v90, v64, v65\n"
"v_cvt_pk_bf16_f32 v91, v66, v67\n"
"v_mfma_f32_32x32x16_bf16 v[52:67], v[92:95], v[4:7], 0\n"
"s_mov_b64 exec, 1\n"
"ds_add_u32 v132, v133\n"
"s_mov_b64 exec, -1\n"
"v_exp_f32 v68, v68\n"
"v_exp_f32 v69, v69\n"
"v_exp_f32 v70, v70\n"
"v_mfma_f32_32x32x16_bf16 v[20:35], v[104:107], v[88:91], v[20:35]\n"
"v_cvt_pk_bf16_f32 v84, v68, v69\n"
"v_exp_f32 v71, v71\n"
"v_exp_f32 v72, v72\n"
"v_cvt_pk_bf16_f32 v85, v70, v71\n"
"v_exp_f32 v73, v73\n"
"v_exp_f32 v74, v74\n"
"v_cvt_pk_bf16_f32 v86, v72, v73\n"
"v_mfma_f32_32x32x16_bf16 v[52:67], v[96:99], v[8:11], v[52:67]\n"
"v_exp_f32 v75, v75\n"
"v_exp_f32 v76, v76\n"
"v_cvt_pk_bf16_f32 v87, v74, v75\n"
"v_exp_f32 v77, v77\n"
"v_exp_f32 v78, v78\n"
"v_cvt_pk_bf16_f32 v88, v76, v77\n"
"v_mfma_f32_32x32x16_bf16 v[36:51], v[108:111], v[84:87], v[36:51]\n"
"ds_read_b128 v[108:111], v114 offset:34112\n"
"v_exp_f32 v79, v79\n"
"v_exp_f32 v80, v80\n"
"v_cvt_pk_bf16_f32 v89, v78, v79\n"
"v_exp_f32 v81, v81\n"
"v_exp_f32 v82, v82\n"
"v_exp_f32 v83, v83\n"
"v_cvt_pk_bf16_f32 v90, v80, v81\n"
"v_cvt_pk_bf16_f32 v91, v82, v83\n"
"v_mfma_f32_32x32x16_bf16 v[68:83], v[92:95], v[12:15], 0\n"
"ds_read_b128 v[92:95], v112 offset:34112\n"
"v_exp_f32 v52, v52\n"
"v_exp_f32 v53, v53\n"
"v_exp_f32 v54, v54\n"
"v_mfma_f32_32x32x16_bf16 v[36:51], v[104:107], v[88:91], v[36:51]\n"
"ds_read_b128 v[104:107], v114 offset:32160\n"
"v_cvt_pk_bf16_f32 v84, v52, v53\n"
"v_exp_f32 v55, v55\n"
"v_exp_f32 v56, v56\n"
"v_cvt_pk_bf16_f32 v85, v54, v55\n"
"v_exp_f32 v57, v57\n"
"v_exp_f32 v58, v58\n"
"v_cvt_pk_bf16_f32 v86, v56, v57\n"
"v_mfma_f32_32x32x16_bf16 v[68:83], v[96:99], v[16:19], v[68:83]\n"
"ds_read_b64 v[96:97], v113 offset:34112\n"
"v_exp_f32 v59, v59\n"
"v_exp_f32 v60, v60\n"
"v_cvt_pk_bf16_f32 v87, v58, v59\n"
"v_exp_f32 v61, v61\n"
"v_exp_f32 v62, v62\n"
"v_cvt_pk_bf16_f32 v88, v60, v61\n"
"v_mfma_f32_32x32x16_bf16 v[20:35], v[100:103], v[84:87], v[20:35]\n"
"v_exp_f32 v63, v63\n"
"v_exp_f32 v64, v64\n"
"v_cvt_pk_bf16_f32 v89, v62, v63\n"
"v_exp_f32 v65, v65\n"
"v_exp_f32 v66, v66\n"
"v_exp_f32 v67, v67\n"
"s_waitcnt lgkmcnt(0)\n"
"v_cvt_pk_bf16_f32 v90, v64, v65\n"
"v_cvt_pk_bf16_f32 v91, v66, v67\n"
"v_mfma_f32_32x32x16_bf16 v[52:67], v[92:95], v[4:7], 0\n"
"v_exp_f32 v68, v68\n"
"v_exp_f32 v69, v69\n"
"v_exp_f32 v70, v70\n"
"v_mfma_f32_32x32x16_bf16 v[20:35], v[104:107], v[88:91], v[20:35]\n"
"v_cvt_pk_bf16_f32 v84, v68, v69\n"
"v_exp_f32 v71, v71\n"
"v_exp_f32 v72, v72\n"
"v_cvt_pk_bf16_f32 v85, v70, v71\n"
"v_exp_f32 v73, v73\n"
"v_exp_f32 v74, v74\n"
"v_cvt_pk_bf16_f32 v86, v72, v73\n"
"v_mfma_f32_32x32x16_bf16 v[52:67], v[96:99], v[8:11], v[52:67]\n"
"v_exp_f32 v75, v75\n"
"v_exp_f32 v76, v76\n"
"v_cvt_pk_bf16_f32 v87, v74, v75\n"
"v_exp_f32 v77, v77\n"
"v_exp_f32 v78, v78\n"
"v_cvt_pk_bf16_f32 v88, v76, v77\n"
"v_mfma_f32_32x32x16_bf16 v[36:51], v[100:103], v[84:87], v[36:51]\n"
"ds_read_b128 v[100:103], v114 offset:36736\n"
"v_exp_f32 v79, v79\n"
"v_exp_f32 v80, v80\n"
"v_cvt_pk_bf16_f32 v89, v78, v79\n"
"v_exp_f32 v81, v81\n"
"v_exp_f32 v82, v82\n"
"v_exp_f32 v83, v83\n"
"v_cvt_pk_bf16_f32 v90, v80, v81\n"
"v_cvt_pk_bf16_f32 v91, v82, v83\n"
"v_mfma_f32_32x32x16_bf16 v[68:83], v[92:95], v[12:15], 0\n"
"ds_read_b128 v[92:95], v112 offset:36736\n"
"v_exp_f32 v52, v52\n"
"v_exp_f32 v53, v53\n"
"v_exp_f32 v54, v54\n"
"v_mfma_f32_32x32x16_bf16 v[36:51], v[104:107], v[88:91], v[36:51]\n"
"ds_read_b128 v[104:107], v114 offset:34784\n"
"v_cvt_pk_bf16_f32 v84, v52, v53\n"
"v_exp_f32 v55, v55\n"
"v_exp_f32 v56, v56\n"
"v_cvt_pk_bf16_f32 v85, v54, v55\n"
"v_exp_f32 v57, v57\n"
"v_exp_f32 v58, v58\n"
"v_cvt_pk_bf16_f32 v86, v56, v57\n"
"v_mfma_f32_32x32x16_bf16 v[68:83], v[96:99], v[16:19], v[68:83]\n"
"ds_read_b64 v[96:97], v113 offset:36736\n"
"v_exp_f32 v59, v59\n"
"v_exp_f32 v60, v60\n"
"v_cvt_pk_bf16_f32 v87, v58, v59\n"
"v_exp_f32 v61, v61\n"
"v_exp_f32 v62, v62\n"
"v_cvt_pk_bf16_f32 v88, v60, v61\n"
"v_mfma_f32_32x32x16_bf16 v[20:35], v[108:111], v[84:87], v[20:35]\n"
"v_exp_f32 v63, v63\n"
"v_exp_f32 v64, v64\n"
"v_cvt_pk_bf16_f32 v89, v62, v63\n"
"v_exp_f32 v65, v65\n"
"v_exp_f32 v66, v66\n"
"v_exp_f32 v67, v67\n"
"s_waitcnt lgkmcnt(0)\n"
"v_cvt_pk_bf16_f32 v90, v64, v65\n"
"v_cvt_pk_bf16_f32 v91, v66, v67\n"
"v_mfma_f32_32x32x16_bf16 v[52:67], v[92:95], v[4:7], 0\n"
"v_exp_f32 v68, v68\n"
"v_exp_f32 v69, v69\n"
"v_exp_f32 v70, v70\n"
"v_mfma_f32_32x32x16_bf16 v[20:35], v[104:107], v[88:91], v[20:35]\n"
"v_cvt_pk_bf16_f32 v84, v68, v69\n"
"v_exp_f32 v71, v71\n"
"v_exp_f32 v72, v72\n"
"v_cvt_pk_bf16_f32 v85, v70, v71\n"
"v_exp_f32 v73, v73\n"
"v_exp_f32 v74, v74\n"
"v_cvt_pk_bf16_f32 v86, v72, v73\n"
"v_mfma_f32_32x32x16_bf16 v[52:67], v[96:99], v[8:11], v[52:67]\n"
"v_exp_f32 v75, v75\n"
"v_exp_f32 v76, v76\n"
"v_cvt_pk_bf16_f32 v87, v74, v75\n"
"v_exp_f32 v77, v77\n"
"v_exp_f32 v78, v78\n"
"v_cvt_pk_bf16_f32 v88, v76, v77\n"
"v_mfma_f32_32x32x16_bf16 v[36:51], v[108:111], v[84:87], v[36:51]\n"
"ds_read_b128 v[108:111], v114 offset:39360\n"
"v_exp_f32 v79, v79\n"
"v_exp_f32 v80, v80\n"
"v_cvt_pk_bf16_f32 v89, v78, v79\n"
"v_exp_f32 v81, v81\n"
"v_exp_f32 v82, v82\n"
"v_exp_f32 v83, v83\n"
"v_cvt_pk_bf16_f32 v90, v80, v81\n"
"v_cvt_pk_bf16_f32 v91, v82, v83\n"
"v_mfma_f32_32x32x16_bf16 v[68:83], v[92:95], v[12:15], 0\n"
"ds_read_b128 v[92:95], v112 offset:39360\n"
"v_exp_f32 v52, v52\n"
"v_exp_f32 v53, v53\n"
"v_exp_f32 v54, v54\n"
"v_mfma_f32_32x32x16_bf16 v[36:51], v[104:107], v[88:91], v[36:51]\n"
"ds_read_b128 v[104:107], v114 offset:37408\n"
"v_cvt_pk_bf16_f32 v84, v52, v53\n"
"v_exp_f32 v55, v55\n"
"v_exp_f32 v56, v56\n"
"v_cvt_pk_bf16_f32 v85, v54, v55\n"
"v_exp_f32 v57, v57\n"
"v_exp_f32 v58, v58\n"
"v_cvt_pk_bf16_f32 v86, v56, v57\n"
"v_mfma_f32_32x32x16_bf16 v[68:83], v[96:99], v[16:19], v[68:83]\n"
"ds_read_b64 v[96:97], v113 offset:39360\n"
"v_exp_f32 v59, v59\n"
"v_exp_f32 v60, v60\n"
"v_cvt_pk_bf16_f32 v87, v58, v59\n"
"v_exp_f32 v61, v61\n"
"v_exp_f32 v62, v62\n"
"v_cvt_pk_bf16_f32 v88, v60, v61\n"
"v_mfma_f32_32x32x16_bf16 v[20:35], v[100:103], v[84:87], v[20:35]\n"
"v_exp_f32 v63, v63\n"
"v_exp_f32 v64, v64\n"
"v_cvt_pk_bf16_f32 v89, v62, v63\n"
"v_exp_f32 v65, v65\n"
"v_exp_f32 v66, v66\n"
"v_exp_f32 v67, v67\n"
"s_waitcnt lgkmcnt(0)\n"
"v_cvt_pk_bf16_f32 v90, v64, v65\n"
"v_cvt_pk_bf16_f32 v91, v66, v67\n"
"v_mfma_f32_32x32x16_bf16 v[52:67], v[92:95], v[4:7], 0\n"
"ds_read_b32 v131, v130\n"
"v_exp_f32 v68, v68\n"
"v_exp_f32 v69, v69\n"
"v_exp_f32 v70, v70\n"
"v_mfma_f32_32x32x16_bf16 v[20:35], v[104:107], v[88:91], v[20:35]\n"
"v_cvt_pk_bf16_f32 v84, v68, v69\n"
"v_exp_f32 v71, v71\n"
"v_exp_f32 v72, v72\n"
"v_cvt_pk_bf16_f32 v85, v70, v71\n"
"v_exp_f32 v73, v73\n"
"v_exp_f32 v74, v74\n"
"v_cvt_pk_bf16_f32 v86, v72, v73\n"
"v_mfma_f32_32x32x16_bf16 v[52:67], v[96:99], v[8:11], v[52:67]\n"
"v_exp_f32 v75, v75\n"
"v_exp_f32 v76, v76\n"
"v_cvt_pk_bf16_f32 v87, v74, v75\n"
"v_exp_f32 v77, v77\n"
"v_exp_f32 v78, v78\n"
"v_cvt_pk_bf16_f32 v88, v76, v77\n"
"v_mfma_f32_32x32x16_bf16 v[36:51], v[100:103], v[84:87], v[36:51]\n"
"s_waitcnt lgkmcnt(0)\n"
"v_cmp_gt_u32 vcc, s70, v131\n"
"s_cbranch_vccnz L_rs12\n"
"L_rdy12:\n"
"s_add_u32 s70, s70, 1\n"
"ds_read_b128 v[100:103], v114 offset:0\n"
"v_exp_f32 v79, v79\n"
"v_exp_f32 v80, v80\n"
"v_cvt_pk_bf16_f32 v89, v78, v79\n"
"v_exp_f32 v81, v81\n"
"v_exp_f32 v82, v82\n"
"v_exp_f32 v83, v83\n"
"v_cvt_pk_bf16_f32 v90, v80, v81\n"
"v_cvt_pk_bf16_f32 v91, v82, v83\n"
"v_mfma_f32_32x32x16_bf16 v[68:83], v[92:95], v[12:15], 0\n"
"ds_read_b128 v[92:95], v112 offset:0\n"
"v_exp_f32 v52, v52\n"
"v_exp_f32 v53, v53\n"
"v_exp_f32 v54, v54\n"
"v_mfma_f32_32x32x16_bf16 v[36:51], v[104:107], v[88:91], v[36:51]\n"
"ds_read_b128 v[104:107], v114 offset:40032\n"
"v_cvt_pk_bf16_f32 v84, v52, v53\n"
"v_exp_f32 v55, v55\n"
"v_exp_f32 v56, v56\n"
"v_cvt_pk_bf16_f32 v85, v54, v55\n"
"v_exp_f32 v57, v57\n"
"v_exp_f32 v58, v58\n"
"v_cvt_pk_bf16_f32 v86, v56, v57\n"
"v_mfma_f32_32x32x16_bf16 v[68:83], v[96:99], v[16:19], v[68:83]\n"
"ds_read_b64 v[96:97], v113 offset:0\n"
"v_exp_f32 v59, v59\n"
"v_exp_f32 v60, v60\n"
"v_cvt_pk_bf16_f32 v87, v58, v59\n"
"v_exp_f32 v61, v61\n"
"v_exp_f32 v62, v62\n"
"v_cvt_pk_bf16_f32 v88, v60, v61\n"
"v_mfma_f32_32x32x16_bf16 v[20:35], v[108:111], v[84:87], v[20:35]\n"
"v_exp_f32 v63, v63\n"
"v_exp_f32 v64, v64\n"
"v_cvt_pk_bf16_f32 v89, v62, v63\n"
"v_exp_f32 v65, v65\n"
"v_exp_f32 v66, v66\n"
"v_exp_f32 v67, v67\n"
"s_waitcnt lgkmcnt(0)\n"
"v_cvt_pk_bf16_f32 v90, v64, v65\n"
"v_cvt_pk_bf16_f32 v91, v66, v67\n"
"v_mfma_f32_32x32x16_bf16 v[52:67], v[92:95], v[4:7], 0\n"
"s_mov_b64 exec, 1\n"
"ds_add_u32 v132, v133\n"
"s_mov_b64 exec, -1\n"
"v_exp_f32 v68, v68\n"
"v_exp_f32 v69, v69\n"
"v_exp_f32 v70, v70\n"
"v_mfma_f32_32x32x16_bf16 v[20:35], v[104:107], v[88:91], v[20:35]\n"
"v_cvt_pk_bf16_f32 v84, v68, v69\n"
"v_exp_f32 v71, v71\n"
"v_exp_f32 v72, v72\n"
"v_cvt_pk_bf16_f32 v85, v70, v71\n"
"v_exp_f32 v73, v73\n"
"v_exp_f32 v74, v74\n"
"v_cvt_pk_bf16_f32 v86, v72, v73\n"
"v_mfma_f32_32x32x16_bf16 v[52:67], v[96:99], v[8:11], v[52:67]\n"
"v_exp_f32 v75, v75\n"
"v_exp_f32 v76, v76\n"
"v_cvt_pk_bf16_f32 v87, v74, v75\n"
"v_exp_f32 v77, v77\n"
"v_exp_f32 v78, v78\n"
"v_cvt_pk_bf16_f32 v88, v76, v77\n"
"v_mfma_f32_32x32x16_bf16 v[36:51], v[108:111], v[84:87], v[36:51]\n"
"ds_read_b128 v[108:111], v114 offset:2624\n"
"v_exp_f32 v79, v79\n"
"v_exp_f32 v80, v80\n"
"v_cvt_pk_bf16_f32 v89, v78, v79\n"
"v_exp_f32 v81, v81\n"
"v_exp_f32 v82, v82\n"
"v_exp_f32 v83, v83\n"
"v_cvt_pk_bf16_f32 v90, v80, v81\n"
"v_cvt_pk_bf16_f32 v91, v82, v83\n"
"s_sub_u32 s71, s71, 1\n"
"s_cmp_lg_u32 s71, 0\n"
"s_cbranch_scc1 L_attn\n"
"s_nop 1\n"
"v_mfma_f32_32x32x16_bf16 v[36:51], v[104:107], v[88:91], v[36:51]\n"
"s_waitcnt vmcnt(0) lgkmcnt(0)\n"
"s_setprio 3\n"
"v_max3_f32 v134, |v20|, |v21|, |v22|\n"
"v_max3_f32 v134, v134, |v23|, |v24|\n"
"v_max3_f32 v134, v134, |v25|, |v26|\n"
"v_max3_f32 v134, v134, |v27|, |v28|\n"
"v_max3_f32 v134, v134, |v29|, |v30|\n"
"v_max_f32 v134, v134, |v31|\n"
"s_nop 3\n"
"v_max3_f32 v135, |v36|, |v37|, |v38|\n"
"v_max3_f32 v135, v135, |v39|, |v40|\n"
"v_max3_f32 v135, v135, |v41|, |v42|\n"
"v_max3_f32 v135, v135, |v43|, |v44|\n"
"v_max3_f32 v135, v135, |v45|, |v46|\n"
"v_max_f32 v135, v135, |v47|\n"
"v_mov_b32 v136, 0\n"
"v_mov_b32 v137, 0\n"
"v_mov_b32 v138, 0\n"
"v_mov_b32 v139, 0\n"
"v_pk_fma_f32 v[136:137], v[20:21], v[138:139], v[136:137]\n"
"v_pk_fma_f32 v[136:137], v[22:23], v[138:139], v[136:137]\n"
"v_pk_fma_f32 v[136:137], v[24:25], v[138:139], v[136:137]\n"
"v_pk_fma_f32 v[136:137], v[26:27], v[138:139], v[136:137]\n"
"v_pk_fma_f32 v[136:137], v[28:29], v[138:139], v[136:137]\n"
"v_pk_fma_f32 v[136:137], v[30:31], v[138:139], v[136:137]\n"
"v_pk_fma_f32 v[136:137], v[36:37], v[138:139], v[136:137]\n"
"v_pk_fma_f32 v[136:137], v[38:39], v[138:139], v[136:137]\n"
"v_pk_fma_f32 v[136:137], v[40:41], v[138:139], v[136:137]\n"
"v_pk_fma_f32 v[136:137], v[42:43], v[138:139], v[136:137]\n"
"v_pk_fma_f32 v[136:137], v[44:45], v[138:139], v[136:137]\n"
"v_pk_fma_f32 v[136:137], v[46:47], v[138:139], v[136:137]\n"
"v_add_f32 v136, v136, v137\n"
"v_max_f32 v134, v134, v135\n"
"s_mov_b32 s60, 0x7b800000\n"
"v_cmp_ngt_f32 vcc, s60, v134\n"
"v_cmp_u_f32 s[62:63], v136, v136\n"
"s_or_b64 vcc, vcc, s[62:63]\n"
"s_cbranch_vccnz L_slow\n"
"L_slowret:\n"
"v_mov_b32 v134, v28\n"
"v_mov_b32 v135, v28\n"
"s_nop 1\n"
"v_permlane32_swap_b32 v134, v135\n"
"v_rcp_f32 v135, v135\n"
"s_nop 0\n"
"v_mul_f32 v20, v20, v135\n"
"v_mul_f32 v21, v21, v135\n"
"v_mul_f32 v22, v22, v135\n"
"v_mul_f32 v23, v23, v135\n"
"v_mul_f32 v24, v24, v135\n"
"v_mul_f32 v25, v25, v135\n"
"v_mul_f32 v26, v26, v135\n"
"v_mul_f32 v27, v27, v135\n"
"v_mul_f32 v28, v28, v135\n"
"v_mul_f32 v29, v29, v135\n"
"v_mul_f32 v30, v30, v135\n"
"v_mul_f32 v31, v31, v135\n"
"v_cvt_pk_bf16_f32 v140, v20, v21\n"
"v_cvt_pk_bf16_f32 v141, v22, v23\n"
"v_cvt_pk_bf16_f32 v142, v24, v25\n"
"v_cvt_pk_bf16_f32 v143, v26, v27\n"
"v_cvt_pk_bf16_f32 v144, v28, v29\n"
"v_cvt_pk_bf16_f32 v145, v30, v31\n"
"v_min_u32 v140, 0xfffffffe, v140\n"
"v_min_u32 v141, 0xfffffffe, v141\n"
"v_min_u32 v142, 0xfffffffe, v142\n"
"v_min_u32 v143, 0xfffffffe, v143\n"
"v_min_u32 v144, 0xfffffffe, v144\n"
"v_min_u32 v145, 0xfffffffe, v145\n"
"buffer_store_dwordx4 v[140:143], v232, s[36:39], s64 offen sc1\n"
"s_mov_b32 exec_hi, 0\n"
"buffer_store_dwordx2 v[144:145], v233, s[36:39], s65 offen sc1\n"
"s_mov_b64 exec, -1\n"
"v_mov_b32 v134, v44\n"
"v_mov_b32 v135, v44\n"
"s_nop 1\n"
"v_permlane32_swap_b32 v134, v135\n"
"v_rcp_f32 v135, v135\n"
"s_nop 0\n"
"v_mul_f32 v36, v36, v135\n"
"v_mul_f32 v37, v37, v135\n"
"v_mul_f32 v38, v38, v135\n"
"v_mul_f32 v39, v39, v135\n"
"v_mul_f32 v40, v40, v135\n"
"v_mul_f32 v41, v41, v135\n"
"v_mul_f32 v42, v42, v135\n"
"v_mul_f32 v43, v43, v135\n"
"v_mul_f32 v44, v44, v135\n"
"v_mul_f32 v45, v45, v135\n"
"v_mul_f32 v46, v46, v135\n"
"v_mul_f32 v47, v47, v135\n"
"v_cvt_pk_bf16_f32 v148, v36, v37\n"
"v_cvt_pk_bf16_f32 v149, v38, v39\n"
"v_cvt_pk_bf16_f32 v150, v40, v41\n"
"v_cvt_pk_bf16_f32 v151, v42, v43\n"
"v_cvt_pk_bf16_f32 v152, v44, v45\n"
"v_cvt_pk_bf16_f32 v153, v46, v47\n"
"v_min_u32 v148, 0xfffffffe, v148\n"
"v_min_u32 v149, 0xfffffffe, v149\n"
"v_min_u32 v150, 0xfffffffe, v150\n"
"v_min_u32 v151, 0xfffffffe, v151\n"
"v_min_u32 v152, 0xfffffffe, v152\n"
"v_min_u32 v153, 0xfffffffe, v153\n"
"s_add_u32 s64, s64, 5120\n"
"buffer_store_dwordx4 v[148:151], v232, s[36:39], s64 offen sc1\n"
"s_mov_b32 exec_hi, 0\n"
"s_add_u32 s65, s65, 5120\n"
"buffer_store_dwordx2 v[152:153], v233, s[36:39], s65 offen sc1\n"
"s_mov_b64 exec, -1\n"
"s_setprio 0\n"
"s_mov_b32 s52, s24\n"
"s_and_b32 s53, s25, 0xffff\n"
"s_mov_b32 s54, 1280\n"
"s_mov_b32 s55, 0x20000\n"
"v_add_u32 v17, 0, %2\n"
"v_mul_u32_u24 v18, 6554, v17\n"
"v_lshrrev_b32 v18, 16, v18\n"
"v_mul_u32_u24 v19, 10, v18\n"
"v_sub_u32 v19, v17, v19\n"
"v_mul_u32_u24 v46, 336, v18\n"
"v_lshl_add_u32 v46, v19, 5, v46\n"
"v_add_u32 v46, 41984, v46\n"
"s_lshl_b32 s60, s30, 6\n"
"v_add_u32 v18, s60, v18\n"
"v_mul_u32_u24 v18, 1280, v18\n"
"s_mul_i32 s60, s31, 160\n"
"v_lshl_add_u32 v19, v19, 4, s60\n"
"v_add_u32 v44, v18, v19\n"
"buffer_load_dwordx4 v[48:51], v44, s[40:43], 0 offen\n"
"buffer_load_dwordx4 v[52:55], v19, s[52:55], 0 offen\n"
"v_add_u32 v17, 512, %2\n"
"v_mul_u32_u24 v18, 6554, v17\n"
"v_lshrrev_b32 v18, 16, v18\n"
"v_mul_u32_u24 v40, 10, v18\n"
"v_sub_u32 v40, v17, v40\n"
"v_mul_u32_u24 v47, 336, v18\n"
"v_lshl_add_u32 v47, v40, 5, v47\n"
"v_add_u32 v47, 41984, v47\n"
"s_lshl_b32 s60, s30, 6\n"
"v_add_u32 v18, s60, v18\n"
"v_mul_u32_u24 v18, 1280, v18\n"
"s_mul_i32 s60, s31, 160\n"
"v_lshl_add_u32 v40, v40, 4, s60\n"
"v_add_u32 v45, v18, v40\n"
"buffer_load_dwordx4 v[56:59], v45, s[40:43], 0 offen\n"
"buffer_load_dwordx4 v[60:63], v40, s[52:55], 0 offen\n"
"s_waitcnt vmcnt(0)\n"
"v_lshlrev_b32 v20, 16, v52\n"
"v_and_b32 v21, 0xffff0000, v52\n"
"v_lshlrev_b32 v22, 16, v53\n"
"v_and_b32 v23, 0xffff0000, v53\n"
"v_lshlrev_b32 v24, 16, v54\n"
"v_and_b32 v25, 0xffff0000, v54\n"
"v_lshlrev_b32 v26, 16, v55\n"
"v_and_b32 v27, 0xffff0000, v55\n"
"v_and_b32 v28, 0xffff0000, v51\n"
"v_lshlrev_b32 v29, 16, v51\n"
"v_add_f32 v55, v28, v27\n"
"v_add_f32 v54, v29, v26\n"
"v_and_b32 v28, 0xffff0000, v50\n"
"v_lshlrev_b32 v29, 16, v50\n"
"v_add_f32 v53, v28, v25\n"
"v_add_f32 v52, v29, v24\n"
"v_and_b32 v28, 0xffff0000, v49\n"
"v_lshlrev_b32 v29, 16, v49\n"
"v_add_f32 v51, v28, v23\n"
"v_add_f32 v50, v29, v22\n"
"v_and_b32 v28, 0xffff0000, v48\n"
"v_lshlrev_b32 v29, 16, v48\n"
"v_add_f32 v49, v28, v21\n"
"v_add_f32 v48, v29, v20\n"
"v_lshlrev_b32 v20, 16, v60\n"
"v_and_b32 v21, 0xffff0000, v60\n"
"v_lshlrev_b32 v22, 16, v61\n"
"v_and_b32 v23, 0xffff0000, v61\n"
"v_lshlrev_b32 v24, 16, v62\n"
"v_and_b32 v25, 0xffff0000, v62\n"
"v_lshlrev_b32 v26, 16, v63\n"
"v_and_b32 v27, 0xffff0000, v63\n"
"v_and_b32 v28, 0xffff0000, v59\n"
"v_lshlrev_b32 v29, 16, v59\n"
"v_add_f32 v63, v28, v27\n"
"v_add_f32 v62, v29, v26\n"
"v_and_b32 v28, 0xffff0000, v58\n"
"v_lshlrev_b32 v29, 16, v58\n"
"v_add_f32 v61, v28, v25\n"
"v_add_f32 v60, v29, v24\n"
"v_and_b32 v28, 0xffff0000, v57\n"
"v_lshlrev_b32 v29, 16, v57\n"
"v_add_f32 v59, v28, v23\n"
"v_add_f32 v58, v29, v22\n"
"v_and_b32 v28, 0xffff0000, v56\n"
"v_lshlrev_b32 v29, 16, v56\n"
"v_add_f32 v57, v28, v21\n"
"v_add_f32 v56, v29, v20\n"
"s_branch L_final\n"
"L_rs9:\n"
"s_mov_b32 s69, 0\n"
"L_rsl9:\n"
"ds_read_b32 v131, v130\n"
"s_waitcnt lgkmcnt(0)\n"
"v_cmp_gt_u32 vcc, s70, v131\n"
"s_cbranch_vccz L_rsx9\n"
"s_add_u32 s69, s69, 1\n"
"s_cmp_lt_u32 s69, 100000\n"
"s_cbranch_scc1 L_rsl9\n"
"L_rsx9:\n"
"s_branch L_rdy9\n"
"L_rs10:\n"
"s_mov_b32 s69, 0\n"
"L_rsl10:\n"
"ds_read_b32 v131, v130\n"
"s_waitcnt lgkmcnt(0)\n"
"v_cmp_gt_u32 vcc, s70, v131\n"
"s_cbranch_vccz L_rsx10\n"
"s_add_u32 s69, s69, 1\n"
"s_cmp_lt_u32 s69, 100000\n"
"s_cbranch_scc1 L_rsl10\n"
"L_rsx10:\n"
"s_branch L_rdy10\n"
"L_rs11:\n"
"s_mov_b32 s69, 0\n"
"L_rsl11:\n"
"ds_read_b32 v131, v130\n"
"s_waitcnt lgkmcnt(0)\n"
"v_cmp_gt_u32 vcc, s70, v131\n"
"s_cbranch_vccz L_rsx11\n"
"s_add_u32 s69, s69, 1\n"
"s_cmp_lt_u32 s69, 100000\n"
"s_cbranch_scc1 L_rsl11\n"
"L_rsx11:\n"
"s_branch L_rdy11\n"
"L_rs12:\n"
"s_mov_b32 s69, 0\n"
"L_rsl12:\n"
"ds_read_b32 v131, v130\n"
"s_waitcnt lgkmcnt(0)\n"
"v_cmp_gt_u32 vcc, s70, v131\n"
"s_cbranch_vccz L_rsx12\n"
"s_add_u32 s69, s69, 1\n"
"s_cmp_lt_u32 s69, 100000\n"
"s_cbranch_scc1 L_rsl12\n"
"L_rsx12:\n"
"s_branch L_rdy12\n"
"L_slow:\n"
"s_mov_b32 exec_hi, 0\n"
"v_mov_b32 v10, 0\n"
"v_mov_b32 v18, 0\n"
"s_mov_b64 exec, -1\n"
"v_mov_b32 v138, 0xff800000\n"
"v_mov_b32 v139, 0xff800000\n"
"s_mov_b32 s62, 65536\n"
"s_mov_b32 s63, 64\n"
"L_slow1:\n"
"buffer_load_dwordx4 v[92:95], v112, s[36:39], s62 offen sc1\n"
"buffer_load_dwordx2 v[96:97], v113, s[36:39], s62 offen sc1\n"
"s_waitcnt vmcnt(0)\n"
"v_mfma_f32_32x32x16_bf16 v[52:67], v[92:95], v[4:7], 0\n"
"v_mfma_f32_32x32x16_bf16 v[52:67], v[96:99], v[8:11], v[52:67]\n"
"v_mfma_f32_32x32x16_bf16 v[68:83], v[92:95], v[12:15], 0\n"
"v_mfma_f32_32x32x16_bf16 v[68:83], v[96:99], v[16:19], v[68:83]\n"
"s_nop 9\n"
"v_max3_f32 v138, v138, v52, v53\n"
"v_max3_f32 v138, v138, v54, v55\n"
"v_max3_f32 v138, v138, v56, v57\n"
"v_max3_f32 v138, v138, v58, v59\n"
"v_max3_f32 v138, v138, v60, v61\n"
"v_max3_f32 v138, v138, v62, v63\n"
"v_max3_f32 v138, v138, v64, v65\n"
"v_max3_f32 v138, v138, v66, v67\n"
"v_max3_f32 v139, v139, v68, v69\n"
"v_max3_f32 v139, v139, v70, v71\n"
"v_max3_f32 v139, v139, v72, v73\n"
"v_max3_f32 v139, v139, v74, v75\n"
"v_max3_f32 v139, v139, v76, v77\n"
"v_max3_f32 v139, v139, v78, v79\n"
"v_max3_f32 v139, v139, v80, v81\n"
"v_max3_f32 v139, v139, v82, v83\n"
"s_add_u32 s62, s62, 2624\n"
"s_sub_u32 s63, s63, 1\n"
"s_cmp_lg_u32 s63, 0\n"
"s_cbranch_scc1 L_slow1\n"
"v_mov_b32 v134, v138\n"
"s_nop 1\n"
"v_permlane32_swap_b32 v138, v134\n"
"v_max_f32 v138, v138, v134\n"
"v_mov_b32 v134, v139\n"
"s_nop 1\n"
"v_permlane32_swap_b32 v139, v134\n"
"v_max_f32 v139, v139, v134\n"
"v_mov_b32 v20, 0\n"
"v_mov_b32 v36, 0\n"
"v_mov_b32 v21, 0\n"
"v_mov_b32 v37, 0\n"
"v_mov_b32 v22, 0\n"
"v_mov_b32 v38, 0\n"
"v_mov_b32 v23, 0\n"
"v_mov_b32 v39, 0\n"
"v_mov_b32 v24, 0\n"
"v_mov_b32 v40, 0\n"
"v_mov_b32 v25, 0\n"
"v_mov_b32 v41, 0\n"
"v_mov_b32 v26, 0\n"
"v_mov_b32 v42, 0\n"
"v_mov_b32 v27, 0\n"
"v_mov_b32 v43, 0\n"
"v_mov_b32 v28, 0\n"
"v_mov_b32 v44, 0\n"
"v_mov_b32 v29, 0\n"
"v_mov_b32 v45, 0\n"
"v_mov_b32 v30, 0\n"
"v_mov_b32 v46, 0\n"
"v_mov_b32 v31, 0\n"
"v_mov_b32 v47, 0\n"
"v_mov_b32 v32, 0\n"
"v_mov_b32 v48, 0\n"
"v_mov_b32 v33, 0\n"
"v_mov_b32 v49, 0\n"
"v_mov_b32 v34, 0\n"
"v_mov_b32 v50, 0\n"
"v_mov_b32 v35, 0\n"
"v_mov_b32 v51, 0\n"
"s_mov_b32 s62, 65536\n"
"s_mov_b32 s63, 64\n"
"L_slow2:\n"
"buffer_load_dwordx4 v[92:95], v112, s[36:39], s62 offen sc1\n"
"buffer_load_dwordx2 v[96:97], v113, s[36:39], s62 offen sc1\n"
"buffer_load_dwordx4 v[100:103], v114, s[36:39], s62 offen sc1\n"
"buffer_load_dwordx4 v[104:107], v114, s[36:39], s62 offen offset:672 sc1\n"
"s_waitcnt vmcnt(0)\n"
"v_mfma_f32_32x32x16_bf16 v[52:67], v[92:95], v[4:7], 0\n"
"v_mfma_f32_32x32x16_bf16 v[52:67], v[96:99], v[8:11], v[52:67]\n"
"v_mfma_f32_32x32x16_bf16 v[68:83], v[92:95], v[12:15], 0\n"
"v_mfma_f32_32x32x16_bf16 v[68:83], v[96:99], v[16:19], v[68:83]\n"
"s_nop 9\n"
"v_sub_f32 v52, v52, v138\n"
"v_sub_f32 v53, v53, v138\n"
"v_sub_f32 v54, v54, v138\n"
"v_sub_f32 v55, v55, v138\n"
"v_sub_f32 v56, v56, v138\n"
"v_sub_f32 v57, v57, v138\n"
"v_sub_f32 v58, v58, v138\n"
"v_sub_f32 v59, v59, v138\n"
"v_sub_f32 v60, v60, v138\n"
"v_sub_f32 v61, v61, v138\n"
"v_sub_f32 v62, v62, v138\n"
"v_sub_f32 v63, v63, v138\n"
"v_sub_f32 v64, v64, v138\n"
"v_sub_f32 v65, v65, v138\n"
"v_sub_f32 v66, v66, v138\n"
"v_sub_f32 v67, v67, v138\n"
"v_exp_f32 v52, v52\n"
"v_exp_f32 v53, v53\n"
"v_exp_f32 v54, v54\n"
"v_exp_f32 v55, v55\n"
"v_exp_f32 v56, v56\n"
"v_exp_f32 v57, v57\n"
"v_exp_f32 v58, v58\n"
"v_exp_f32 v59, v59\n"
"v_exp_f32 v60, v60\n"
"v_exp_f32 v61, v61\n"
"v_exp_f32 v62, v62\n"
"v_exp_f32 v63, v63\n"
"v_exp_f32 v64, v64\n"
"v_exp_f32 v65, v65\n"
"v_exp_f32 v66, v66\n"
"v_exp_f32 v67, v67\n"
"v_cvt_pk_bf16_f32 v84, v52, v53\n"
"v_cvt_pk_bf16_f32 v85, v54, v55\n"
"v_cvt_pk_bf16_f32 v86, v56, v57\n"
"v_cvt_pk_bf16_f32 v87, v58, v59\n"
"v_cvt_pk_bf16_f32 v88, v60, v61\n"
"v_cvt_pk_bf16_f32 v89, v62, v63\n"
"v_cvt_pk_bf16_f32 v90, v64, v65\n"
"v_cvt_pk_bf16_f32 v91, v66, v67\n"
"v_mfma_f32_32x32x16_bf16 v[20:35], v[100:103], v[84:87], v[20:35]\n"
"s_nop 0\n"
"v_mfma_f32_32x32x16_bf16 v[20:35], v[104:107], v[88:91], v[20:35]\n"
"v_sub_f32 v68, v68, v139\n"
"v_sub_f32 v69, v69, v139\n"
"v_sub_f32 v70, v70, v139\n"
"v_sub_f32 v71, v71, v139\n"
"v_sub_f32 v72, v72, v139\n"
"v_sub_f32 v73, v73, v139\n"
"v_sub_f32 v74, v74, v139\n"
"v_sub_f32 v75, v75, v139\n"
"v_sub_f32 v76, v76, v139\n"
"v_sub_f32 v77, v77, v139\n"
"v_sub_f32 v78, v78, v139\n"
"v_sub_f32 v79, v79, v139\n"
"v_sub_f32 v80, v80, v139\n"
"v_sub_f32 v81, v81, v139\n"
"v_sub_f32 v82, v82, v139\n"
"v_sub_f32 v83, v83, v139\n"
"v_exp_f32 v68, v68\n"
"v_exp_f32 v69, v69\n"
"v_exp_f32 v70, v70\n"
"v_exp_f32 v71, v71\n"
"v_exp_f32 v72, v72\n"
"v_exp_f32 v73, v73\n"
"v_exp_f32 v74, v74\n"
"v_exp_f32 v75, v75\n"
"v_exp_f32 v76, v76\n"
"v_exp_f32 v77, v77\n"
"v_exp_f32 v78, v78\n"
"v_exp_f32 v79, v79\n"
"v_exp_f32 v80, v80\n"
"v_exp_f32 v81, v81\n"
"v_exp_f32 v82, v82\n"
"v_exp_f32 v83, v83\n"
"v_cvt_pk_bf16_f32 v84, v68, v69\n"
"v_cvt_pk_bf16_f32 v85, v70, v71\n"
"v_cvt_pk_bf16_f32 v86, v72, v73\n"
"v_cvt_pk_bf16_f32 v87, v74, v75\n"
"v_cvt_pk_bf16_f32 v88, v76, v77\n"
"v_cvt_pk_bf16_f32 v89, v78, v79\n"
"v_cvt_pk_bf16_f32 v90, v80, v81\n"
"v_cvt_pk_bf16_f32 v91, v82, v83\n"
"v_mfma_f32_32x32x16_bf16 v[36:51], v[100:103], v[84:87], v[36:51]\n"
"s_nop 0\n"
"v_mfma_f32_32x32x16_bf16 v[36:51], v[104:107], v[88:91], v[36:51]\n"
"s_add_u32 s62, s62, 2624\n"
"s_sub_u32 s63, s63, 1\n"
"s_cmp_lg_u32 s63, 0\n"
"s_cbranch_scc1 L_slow2\n"
"s_nop 15\n"
"s_branch L_slowret\n"
"L_aux:\n"
"s_sub_u32 s34, s7, 4\n"
"s_mul_i32 s35, s34, 9216\n"
"s_add_u32 s35, s35, 41984\n"
"s_mov_b32 s44, s10\n"
"s_and_b32 s45, s11, 0xffff\n"
"s_mov_b32 s46, 819200\n"
"s_mov_b32 s47, 0x20000\n"
"s_mul_i32 s60, s31, 160\n"
"v_add_u32 v240, 0, v1\n"
"v_mul_u32_u24 v241, 6554, v240\n"
"v_lshrrev_b32 v241, 16, v241\n"
"v_mul_u32_u24 v242, 10, v241\n"
"v_sub_u32 v242, v240, v242\n"
"v_and_b32 v243, 1, v241\n"
"v_lshlrev_b32 v243, 2, v243\n"
"v_bfe_u32 v244, v241, 1, 2\n"
"v_or_b32 v243, v243, v244\n"
"v_and_b32 v241, 0x18, v241\n"
"v_or_b32 v241, v241, v243\n"
"v_mul_u32_u24 v241, 1280, v241\n"
"v_lshl_add_u32 v241, v242, 4, v241\n"
"v_add_u32 v53, s60, v241\n"
"v_add_u32 v240, 64, v1\n"
"v_mul_u32_u24 v241, 6554, v240\n"
"v_lshrrev_b32 v241, 16, v241\n"
"v_mul_u32_u24 v242, 10, v241\n"
"v_sub_u32 v242, v240, v242\n"
"v_and_b32 v243, 1, v241\n"
"v_lshlrev_b32 v243, 2, v243\n"
"v_bfe_u32 v244, v241, 1, 2\n"
"v_or_b32 v243, v243, v244\n"
"v_and_b32 v241, 0x18, v241\n"
"v_or_b32 v241, v241, v243\n"
"v_mul_u32_u24 v241, 1280, v241\n"
"v_lshl_add_u32 v241, v242, 4, v241\n"
"v_add_u32 v54, s60, v241\n"
"v_add_u32 v240, 128, v1\n"
"v_mul_u32_u24 v241, 6554, v240\n"
"v_lshrrev_b32 v241, 16, v241\n"
"v_mul_u32_u24 v242, 10, v241\n"
"v_sub_u32 v242, v240, v242\n"
"v_and_b32 v243, 1, v241\n"
"v_lshlrev_b32 v243, 2, v243\n"
"v_bfe_u32 v244, v241, 1, 2\n"
"v_or_b32 v243, v243, v244\n"
"v_and_b32 v241, 0x18, v241\n"
"v_or_b32 v241, v241, v243\n"
"v_mul_u32_u24 v241, 1280, v241\n"
"v_lshl_add_u32 v241, v242, 4, v241\n"
"v_add_u32 v55, s60, v241\n"
"v_add_u32 v240, 192, v1\n"
"v_mul_u32_u24 v241, 6554, v240\n"
"v_lshrrev_b32 v241, 16, v241\n"
"v_mul_u32_u24 v242, 10, v241\n"
"v_sub_u32 v242, v240, v242\n"
"v_and_b32 v243, 1, v241\n"
"v_lshlrev_b32 v243, 2, v243\n"
"v_bfe_u32 v244, v241, 1, 2\n"
"v_or_b32 v243, v243, v244\n"
"v_and_b32 v241, 0x18, v241\n"
"v_or_b32 v241, v241, v243\n"
"v_mul_u32_u24 v241, 1280, v241\n"
"v_lshl_add_u32 v241, v242, 4, v241\n"
"v_add_u32 v56, s60, v241\n"
"v_add_u32 v240, 256, v1\n"
"v_mul_u32_u24 v241, 6554, v240\n"
"v_lshrrev_b32 v241, 16, v241\n"
"v_mul_u32_u24 v242, 10, v241\n"
"v_sub_u32 v242, v240, v242\n"
"v_and_b32 v243, 1, v241\n"
"v_lshlrev_b32 v243, 2, v243\n"
"v_bfe_u32 v244, v241, 1, 2\n"
"v_or_b32 v243, v243, v244\n"
"v_and_b32 v241, 0x18, v241\n"
"v_or_b32 v241, v241, v243\n"
"v_mul_u32_u24 v241, 1280, v241\n"
"v_lshl_add_u32 v241, v242, 4, v241\n"
"v_add_u32 v57, s60, v241\n"
"s_barrier\n"
"s_cmp_lg_u32 s34, 0\n"
"s_cbranch_scc1 L_nofe\n"
"s_lshl_b32 s60, s6, 3\n"
"s_add_u32 s60, s60, 36864\n"
"v_mov_b32 v60, s32\n"
"v_mov_b32 v61, s33\n"
"v_mul_u32_u24 v58, 0, v1\n"
"s_mov_b64 exec, 0x1\n"
"buffer_store_dwordx2 v[60:61], v58, s[36:39], s60 offen sc1\n"
"s_mov_b64 exec, -1\n"
"L_nofe:\n"
"s_add_u32 s66, s34, 0\n"
"s_mul_i32 s62, s66, 40960\n"
"s_mul_i32 s63, s66, 5120\n"
"s_add_u32 s61, s63, 0\n"
"s_mov_b32 m0, s61\n"
"s_nop 0\n"
"buffer_load_dwordx4 v53, s[44:47], s62 offen lds\n"
"s_add_u32 s61, s63, 1024\n"
"s_mov_b32 m0, s61\n"
"s_nop 0\n"
"buffer_load_dwordx4 v54, s[44:47], s62 offen lds\n"
"s_add_u32 s61, s63, 2048\n"
"s_mov_b32 m0, s61\n"
"s_nop 0\n"
"buffer_load_dwordx4 v55, s[44:47], s62 offen lds\n"
"s_add_u32 s61, s63, 3072\n"
"s_mov_b32 m0, s61\n"
"s_nop 0\n"
"buffer_load_dwordx4 v56, s[44:47], s62 offen lds\n"
"s_add_u32 s61, s63, 4096\n"
"s_mov_b32 m0, s61\n"
"s_nop 0\n"
"buffer_load_dwordx4 v57, s[44:47], s62 offen lds\n"
"s_add_u32 s66, s34, 4\n"
"s_mul_i32 s62, s66, 40960\n"
"s_mul_i32 s63, s66, 5120\n"
"s_add_u32 s61, s63, 0\n"
"s_mov_b32 m0, s61\n"
"s_nop 0\n"
"buffer_load_dwordx4 v53, s[44:47], s62 offen lds\n"
"s_add_u32 s61, s63, 1024\n"
"s_mov_b32 m0, s61\n"
"s_nop 0\n"
"buffer_load_dwordx4 v54, s[44:47], s62 offen lds\n"
"s_add_u32 s61, s63, 2048\n"
"s_mov_b32 m0, s61\n"
"s_nop 0\n"
"buffer_load_dwordx4 v55, s[44:47], s62 offen lds\n"
"s_add_u32 s61, s63, 3072\n"
"s_mov_b32 m0, s61\n"
"s_nop 0\n"
"buffer_load_dwordx4 v56, s[44:47], s62 offen lds\n"
"s_add_u32 s61, s63, 4096\n"
"s_mov_b32 m0, s61\n"
"s_nop 0\n"
"buffer_load_dwordx4 v57, s[44:47], s62 offen lds\n"
"s_add_u32 s66, s34, 8\n"
"s_mul_i32 s62, s66, 40960\n"
"buffer_load_dwordx4 v[64:67], v53, s[44:47], s62 offen\n"
"buffer_load_dwordx4 v[68:71], v54, s[44:47], s62 offen\n"
"buffer_load_dwordx4 v[72:75], v55, s[44:47], s62 offen\n"
"buffer_load_dwordx4 v[76:79], v56, s[44:47], s62 offen\n"
"buffer_load_dwordx4 v[80:83], v57, s[44:47], s62 offen\n"
"s_add_u32 s66, s34, 12\n"
"s_mul_i32 s62, s66, 40960\n"
"buffer_load_dwordx4 v[84:87], v53, s[44:47], s62 offen\n"
"buffer_load_dwordx4 v[88:91], v54, s[44:47], s62 offen\n"
"buffer_load_dwordx4 v[92:95], v55, s[44:47], s62 offen\n"
"buffer_load_dwordx4 v[96:99], v56, s[44:47], s62 offen\n"
"buffer_load_dwordx4 v[100:103], v57, s[44:47], s62 offen\n"
"s_mov_b32 s60, 0x3f803f80\n"
"v_mov_b32 v240, 0\n"
"v_mov_b32 v241, 0\n"
"v_mov_b32 v242, 0\n"
"v_mov_b32 v243, 0\n"
"s_waitcnt vmcnt(29)\n"
"v_dot2_f32_bf16 v240, v8, s60, v240\n"
"v_dot2_f32_bf16 v242, v8, v8, v242\n"
"v_dot2_f32_bf16 v241, v9, s60, v241\n"
"v_dot2_f32_bf16 v243, v9, v9, v243\n"
"v_dot2_f32_bf16 v240, v10, s60, v240\n"
"v_dot2_f32_bf16 v242, v10, v10, v242\n"
"v_dot2_f32_bf16 v241, v11, s60, v241\n"
"v_dot2_f32_bf16 v243, v11, v11, v243\n"
"s_waitcnt vmcnt(28)\n"
"v_dot2_f32_bf16 v240, v12, s60, v240\n"
"v_dot2_f32_bf16 v242, v12, v12, v242\n"
"v_dot2_f32_bf16 v241, v13, s60, v241\n"
"v_dot2_f32_bf16 v243, v13, v13, v243\n"
"v_dot2_f32_bf16 v240, v14, s60, v240\n"
"v_dot2_f32_bf16 v242, v14, v14, v242\n"
"v_dot2_f32_bf16 v241, v15, s60, v241\n"
"v_dot2_f32_bf16 v243, v15, v15, v243\n"
"s_waitcnt vmcnt(27)\n"
"v_dot2_f32_bf16 v240, v16, s60, v240\n"
"v_dot2_f32_bf16 v242, v16, v16, v242\n"
"v_dot2_f32_bf16 v241, v17, s60, v241\n"
"v_dot2_f32_bf16 v243, v17, v17, v243\n"
"v_dot2_f32_bf16 v240, v18, s60, v240\n"
"v_dot2_f32_bf16 v242, v18, v18, v242\n"
"v_dot2_f32_bf16 v241, v19, s60, v241\n"
"v_dot2_f32_bf16 v243, v19, v19, v243\n"
"s_waitcnt vmcnt(26)\n"
"v_dot2_f32_bf16 v240, v20, s60, v240\n"
"v_dot2_f32_bf16 v242, v20, v20, v242\n"
"v_dot2_f32_bf16 v241, v21, s60, v241\n"
"v_dot2_f32_bf16 v243, v21, v21, v243\n"
"v_dot2_f32_bf16 v240, v22, s60, v240\n"
"v_dot2_f32_bf16 v242, v22, v22, v242\n"
"v_dot2_f32_bf16 v241, v23, s60, v241\n"
"v_dot2_f32_bf16 v243, v23, v23, v243\n"
"s_waitcnt vmcnt(25)\n"
"v_dot2_f32_bf16 v240, v24, s60, v240\n"
"v_dot2_f32_bf16 v242, v24, v24, v242\n"
"v_dot2_f32_bf16 v241, v25, s60, v241\n"
"v_dot2_f32_bf16 v243, v25, v25, v243\n"
"v_dot2_f32_bf16 v240, v26, s60, v240\n"
"v_dot2_f32_bf16 v242, v26, v26, v242\n"
"v_dot2_f32_bf16 v241, v27, s60, v241\n"
"v_dot2_f32_bf16 v243, v27, v27, v243\n"
"s_waitcnt vmcnt(24)\n"
"v_dot2_f32_bf16 v240, v28, s60, v240\n"
"v_dot2_f32_bf16 v242, v28, v28, v242\n"
"v_dot2_f32_bf16 v241, v29, s60, v241\n"
"v_dot2_f32_bf16 v243, v29, v29, v243\n"
"v_dot2_f32_bf16 v240, v30, s60, v240\n"
"v_dot2_f32_bf16 v242, v30, v30, v242\n"
"v_dot2_f32_bf16 v241, v31, s60, v241\n"
"v_dot2_f32_bf16 v243, v31, v31, v243\n"
"s_waitcnt vmcnt(23)\n"
"v_dot2_f32_bf16 v240, v32, s60, v240\n"
"v_dot2_f32_bf16 v242, v32, v32, v242\n"
"v_dot2_f32_bf16 v241, v33, s60, v241\n"
"v_dot2_f32_bf16 v243, v33, v33, v243\n"
"v_dot2_f32_bf16 v240, v34, s60, v240\n"
"v_dot2_f32_bf16 v242, v34, v34, v242\n"
"v_dot2_f32_bf16 v241, v35, s60, v241\n"
"v_dot2_f32_bf16 v243, v35, v35, v243\n"
"s_waitcnt vmcnt(22)\n"
"v_dot2_f32_bf16 v240, v36, s60, v240\n"
"v_dot2_f32_bf16 v242, v36, v36, v242\n"
"v_dot2_f32_bf16 v241, v37, s60, v241\n"
"v_dot2_f32_bf16 v243, v37, v37, v243\n"
"v_dot2_f32_bf16 v240, v38, s60, v240\n"
"v_dot2_f32_bf16 v242, v38, v38, v242\n"
"v_dot2_f32_bf16 v241, v39, s60, v241\n"
"v_dot2_f32_bf16 v243, v39, v39, v243\n"
"s_waitcnt vmcnt(21)\n"
"v_dot2_f32_bf16 v240, v40, s60, v240\n"
"v_dot2_f32_bf16 v242, v40, v40, v242\n"
"v_dot2_f32_bf16 v241, v41, s60, v241\n"
"v_dot2_f32_bf16 v243, v41, v41, v243\n"
"v_dot2_f32_bf16 v240, v42, s60, v240\n"
"v_dot2_f32_bf16 v242, v42, v42, v242\n"
"v_dot2_f32_bf16 v241, v43, s60, v241\n"
"v_dot2_f32_bf16 v243, v43, v43, v243\n"
"s_waitcnt vmcnt(20)\n"
"v_dot2_f32_bf16 v240, v44, s60, v240\n"
"v_dot2_f32_bf16 v242, v44, v44, v242\n"
"v_dot2_f32_bf16 v241, v45, s60, v241\n"
"v_dot2_f32_bf16 v243, v45, v45, v243\n"
"v_dot2_f32_bf16 v240, v46, s60, v240\n"
"v_dot2_f32_bf16 v242, v46, v46, v242\n"
"v_dot2_f32_bf16 v241, v47, s60, v241\n"
"v_dot2_f32_bf16 v243, v47, v47, v243\n"
"s_nop 1\n"
"v_add_f32 v240, v240, v241\n"
"v_add_f32 v242, v242, v243\n"
"s_nop 0\n"
"v_add_f32_dpp v240, v240, v240 quad_perm:[1,0,3,2] row_mask:0xf bank_mask:0xf\n"
"v_add_f32_dpp v242, v242, v242 quad_perm:[1,0,3,2] row_mask:0xf bank_mask:0xf\n"
"s_nop 0\n"
"v_add_f32_dpp v240, v240, v240 quad_perm:[2,3,0,1] row_mask:0xf bank_mask:0xf\n"
"v_add_f32_dpp v242, v242, v242 quad_perm:[2,3,0,1] row_mask:0xf bank_mask:0xf\n"
"s_nop 0\n"
"v_add_f32_dpp v240, v240, v240 row_half_mirror row_mask:0xf bank_mask:0xf\n"
"v_add_f32_dpp v242, v242, v242 row_half_mirror row_mask:0xf bank_mask:0xf\n"
"v_mul_f32 v241, 0x3acccccd, v240\n"
"v_mul_f32 v243, 0x3acccccd, v242\n"
"v_fma_f32 v243, -v241, v241, v243\n"
"v_max_f32 v243, 0, v243\n"
"v_add_f32 v243, 0x3727c5ac, v243\n"
"v_rsq_f32 v244, v243\n"
"s_nop 0\n"
"v_mul_f32 v246, v241, v244\n"
"v_sub_f32 v246, 0, v246\n"
"v_lshlrev_b32 v248, 16, v8\n"
"v_and_b32 v249, 0xffff0000, v8\n"
"v_pk_fma_f32 v[248:249], v[248:249], v[244:245], v[246:247] op_sel_hi:[1,0,0]\n"
"v_cvt_pk_bf16_f32 v8, v248, v249\n"
"v_lshlrev_b32 v250, 16, v9\n"
"v_and_b32 v251, 0xffff0000, v9\n"
"v_pk_fma_f32 v[250:251], v[250:251], v[244:245], v[246:247] op_sel_hi:[1,0,0]\n"
"v_cvt_pk_bf16_f32 v9, v250, v251\n"
"v_lshlrev_b32 v248, 16, v10\n"
"v_and_b32 v249, 0xffff0000, v10\n"
"v_pk_fma_f32 v[248:249], v[248:249], v[244:245], v[246:247] op_sel_hi:[1,0,0]\n"
"v_cvt_pk_bf16_f32 v10, v248, v249\n"
"v_lshlrev_b32 v250, 16, v11\n"
"v_and_b32 v251, 0xffff0000, v11\n"
"v_pk_fma_f32 v[250:251], v[250:251], v[244:245], v[246:247] op_sel_hi:[1,0,0]\n"
"v_cvt_pk_bf16_f32 v11, v250, v251\n"
"ds_write_b128 v7, v[8:11] offset:0\n"
"v_lshlrev_b32 v248, 16, v12\n"
"v_and_b32 v249, 0xffff0000, v12\n"
"v_pk_fma_f32 v[248:249], v[248:249], v[244:245], v[246:247] op_sel_hi:[1,0,0]\n"
"v_cvt_pk_bf16_f32 v12, v248, v249\n"
"v_lshlrev_b32 v250, 16, v13\n"
"v_and_b32 v251, 0xffff0000, v13\n"
"v_pk_fma_f32 v[250:251], v[250:251], v[244:245], v[246:247] op_sel_hi:[1,0,0]\n"
"v_cvt_pk_bf16_f32 v13, v250, v251\n"
"v_lshlrev_b32 v248, 16, v14\n"
"v_and_b32 v249, 0xffff0000, v14\n"
"v_pk_fma_f32 v[248:249], v[248:249], v[244:245], v[246:247] op_sel_hi:[1,0,0]\n"
"v_cvt_pk_bf16_f32 v14, v248, v249\n"
"v_lshlrev_b32 v250, 16, v15\n"
"v_and_b32 v251, 0xffff0000, v15\n"
"v_pk_fma_f32 v[250:251], v[250:251], v[244:245], v[246:247] op_sel_hi:[1,0,0]\n"
"v_cvt_pk_bf16_f32 v15, v250, v251\n"
"ds_write_b128 v7, v[12:15] offset:128\n"
"v_lshlrev_b32 v248, 16, v16\n"
"v_and_b32 v249, 0xffff0000, v16\n"
"v_pk_fma_f32 v[248:249], v[248:249], v[244:245], v[246:247] op_sel_hi:[1,0,0]\n"
"v_cvt_pk_bf16_f32 v16, v248, v249\n"
"v_lshlrev_b32 v250, 16, v17\n"
"v_and_b32 v251, 0xffff0000, v17\n"
"v_pk_fma_f32 v[250:251], v[250:251], v[244:245], v[246:247] op_sel_hi:[1,0,0]\n"
"v_cvt_pk_bf16_f32 v17, v250, v251\n"
"v_lshlrev_b32 v248, 16, v18\n"
"v_and_b32 v249, 0xffff0000, v18\n"
"v_pk_fma_f32 v[248:249], v[248:249], v[244:245], v[246:247] op_sel_hi:[1,0,0]\n"
"v_cvt_pk_bf16_f32 v18, v248, v249\n"
"v_lshlrev_b32 v250, 16, v19\n"
"v_and_b32 v251, 0xffff0000, v19\n"
"v_pk_fma_f32 v[250:251], v[250:251], v[244:245], v[246:247] op_sel_hi:[1,0,0]\n"
"v_cvt_pk_bf16_f32 v19, v250, v251\n"
"ds_write_b128 v7, v[16:19] offset:256\n"
"v_lshlrev_b32 v248, 16, v20\n"
"v_and_b32 v249, 0xffff0000, v20\n"
"v_pk_fma_f32 v[248:249], v[248:249], v[244:245], v[246:247] op_sel_hi:[1,0,0]\n"
"v_cvt_pk_bf16_f32 v20, v248, v249\n"
"v_lshlrev_b32 v250, 16, v21\n"
"v_and_b32 v251, 0xffff0000, v21\n"
"v_pk_fma_f32 v[250:251], v[250:251], v[244:245], v[246:247] op_sel_hi:[1,0,0]\n"
"v_cvt_pk_bf16_f32 v21, v250, v251\n"
"v_lshlrev_b32 v248, 16, v22\n"
"v_and_b32 v249, 0xffff0000, v22\n"
"v_pk_fma_f32 v[248:249], v[248:249], v[244:245], v[246:247] op_sel_hi:[1,0,0]\n"
"v_cvt_pk_bf16_f32 v22, v248, v249\n"
"v_lshlrev_b32 v250, 16, v23\n"
"v_and_b32 v251, 0xffff0000, v23\n"
"v_pk_fma_f32 v[250:251], v[250:251], v[244:245], v[246:247] op_sel_hi:[1,0,0]\n"
"v_cvt_pk_bf16_f32 v23, v250, v251\n"
"ds_write_b128 v7, v[20:23] offset:384\n"
"v_lshlrev_b32 v248, 16, v24\n"
"v_and_b32 v249, 0xffff0000, v24\n"
"v_pk_fma_f32 v[248:249], v[248:249], v[244:245], v[246:247] op_sel_hi:[1,0,0]\n"
"v_cvt_pk_bf16_f32 v24, v248, v249\n"
"v_lshlrev_b32 v250, 16, v25\n"
"v_and_b32 v251, 0xffff0000, v25\n"
"v_pk_fma_f32 v[250:251], v[250:251], v[244:245], v[246:247] op_sel_hi:[1,0,0]\n"
"v_cvt_pk_bf16_f32 v25, v250, v251\n"
"v_lshlrev_b32 v248, 16, v26\n"
"v_and_b32 v249, 0xffff0000, v26\n"
"v_pk_fma_f32 v[248:249], v[248:249], v[244:245], v[246:247] op_sel_hi:[1,0,0]\n"
"v_cvt_pk_bf16_f32 v26, v248, v249\n"
"v_lshlrev_b32 v250, 16, v27\n"
"v_and_b32 v251, 0xffff0000, v27\n"
"v_pk_fma_f32 v[250:251], v[250:251], v[244:245], v[246:247] op_sel_hi:[1,0,0]\n"
"v_cvt_pk_bf16_f32 v27, v250, v251\n"
"ds_write_b128 v7, v[24:27] offset:512\n"
"v_lshlrev_b32 v248, 16, v28\n"
"v_and_b32 v249, 0xffff0000, v28\n"
"v_pk_fma_f32 v[248:249], v[248:249], v[244:245], v[246:247] op_sel_hi:[1,0,0]\n"
"v_cvt_pk_bf16_f32 v28, v248, v249\n"
"v_lshlrev_b32 v250, 16, v29\n"
"v_and_b32 v251, 0xffff0000, v29\n"
"v_pk_fma_f32 v[250:251], v[250:251], v[244:245], v[246:247] op_sel_hi:[1,0,0]\n"
"v_cvt_pk_bf16_f32 v29, v250, v251\n"
"v_lshlrev_b32 v248, 16, v30\n"
"v_and_b32 v249, 0xffff0000, v30\n"
"v_pk_fma_f32 v[248:249], v[248:249], v[244:245], v[246:247] op_sel_hi:[1,0,0]\n"
"v_cvt_pk_bf16_f32 v30, v248, v249\n"
"v_lshlrev_b32 v250, 16, v31\n"
"v_and_b32 v251, 0xffff0000, v31\n"
"v_pk_fma_f32 v[250:251], v[250:251], v[244:245], v[246:247] op_sel_hi:[1,0,0]\n"
"v_cvt_pk_bf16_f32 v31, v250, v251\n"
"ds_write_b128 v7, v[28:31] offset:640\n"
"v_lshlrev_b32 v248, 16, v32\n"
"v_and_b32 v249, 0xffff0000, v32\n"
"v_pk_fma_f32 v[248:249], v[248:249], v[244:245], v[246:247] op_sel_hi:[1,0,0]\n"
"v_cvt_pk_bf16_f32 v32, v248, v249\n"
"v_lshlrev_b32 v250, 16, v33\n"
"v_and_b32 v251, 0xffff0000, v33\n"
"v_pk_fma_f32 v[250:251], v[250:251], v[244:245], v[246:247] op_sel_hi:[1,0,0]\n"
"v_cvt_pk_bf16_f32 v33, v250, v251\n"
"v_lshlrev_b32 v248, 16, v34\n"
"v_and_b32 v249, 0xffff0000, v34\n"
"v_pk_fma_f32 v[248:249], v[248:249], v[244:245], v[246:247] op_sel_hi:[1,0,0]\n"
"v_cvt_pk_bf16_f32 v34, v248, v249\n"
"v_lshlrev_b32 v250, 16, v35\n"
"v_and_b32 v251, 0xffff0000, v35\n"
"v_pk_fma_f32 v[250:251], v[250:251], v[244:245], v[246:247] op_sel_hi:[1,0,0]\n"
"v_cvt_pk_bf16_f32 v35, v250, v251\n"
"ds_write_b128 v7, v[32:35] offset:768\n"
"v_lshlrev_b32 v248, 16, v36\n"
"v_and_b32 v249, 0xffff0000, v36\n"
"v_pk_fma_f32 v[248:249], v[248:249], v[244:245], v[246:247] op_sel_hi:[1,0,0]\n"
"v_cvt_pk_bf16_f32 v36, v248, v249\n"
"v_lshlrev_b32 v250, 16, v37\n"
"v_and_b32 v251, 0xffff0000, v37\n"
"v_pk_fma_f32 v[250:251], v[250:251], v[244:245], v[246:247] op_sel_hi:[1,0,0]\n"
"v_cvt_pk_bf16_f32 v37, v250, v251\n"
"v_lshlrev_b32 v248, 16, v38\n"
"v_and_b32 v249, 0xffff0000, v38\n"
"v_pk_fma_f32 v[248:249], v[248:249], v[244:245], v[246:247] op_sel_hi:[1,0,0]\n"
"v_cvt_pk_bf16_f32 v38, v248, v249\n"
"v_lshlrev_b32 v250, 16, v39\n"
"v_and_b32 v251, 0xffff0000, v39\n"
"v_pk_fma_f32 v[250:251], v[250:251], v[244:245], v[246:247] op_sel_hi:[1,0,0]\n"
"v_cvt_pk_bf16_f32 v39, v250, v251\n"
"ds_write_b128 v7, v[36:39] offset:896\n"
"v_lshlrev_b32 v248, 16, v40\n"
"v_and_b32 v249, 0xffff0000, v40\n"
"v_pk_fma_f32 v[248:249], v[248:249], v[244:245], v[246:247] op_sel_hi:[1,0,0]\n"
"v_cvt_pk_bf16_f32 v40, v248, v249\n"
"v_lshlrev_b32 v250, 16, v41\n"
"v_and_b32 v251, 0xffff0000, v41\n"
"v_pk_fma_f32 v[250:251], v[250:251], v[244:245], v[246:247] op_sel_hi:[1,0,0]\n"
"v_cvt_pk_bf16_f32 v41, v250, v251\n"
"v_lshlrev_b32 v248, 16, v42\n"
"v_and_b32 v249, 0xffff0000, v42\n"
"v_pk_fma_f32 v[248:249], v[248:249], v[244:245], v[246:247] op_sel_hi:[1,0,0]\n"
"v_cvt_pk_bf16_f32 v42, v248, v249\n"
"v_lshlrev_b32 v250, 16, v43\n"
"v_and_b32 v251, 0xffff0000, v43\n"
"v_pk_fma_f32 v[250:251], v[250:251], v[244:245], v[246:247] op_sel_hi:[1,0,0]\n"
"v_cvt_pk_bf16_f32 v43, v250, v251\n"
"ds_write_b128 v7, v[40:43] offset:1024\n"
"v_lshlrev_b32 v248, 16, v44\n"
"v_and_b32 v249, 0xffff0000, v44\n"
"v_pk_fma_f32 v[248:249], v[248:249], v[244:245], v[246:247] op_sel_hi:[1,0,0]\n"
"v_cvt_pk_bf16_f32 v44, v248, v249\n"
"v_lshlrev_b32 v250, 16, v45\n"
"v_and_b32 v251, 0xffff0000, v45\n"
"v_pk_fma_f32 v[250:251], v[250:251], v[244:245], v[246:247] op_sel_hi:[1,0,0]\n"
"v_cvt_pk_bf16_f32 v45, v250, v251\n"
"v_lshlrev_b32 v248, 16, v46\n"
"v_and_b32 v249, 0xffff0000, v46\n"
"v_pk_fma_f32 v[248:249], v[248:249], v[244:245], v[246:247] op_sel_hi:[1,0,0]\n"
"v_cvt_pk_bf16_f32 v46, v248, v249\n"
"v_lshlrev_b32 v250, 16, v47\n"
"v_and_b32 v251, 0xffff0000, v47\n"
"v_pk_fma_f32 v[250:251], v[250:251], v[244:245], v[246:247] op_sel_hi:[1,0,0]\n"
"v_cvt_pk_bf16_f32 v47, v250, v251\n"
"ds_write_b128 v7, v[44:47] offset:1152\n"
"s_waitcnt lgkmcnt(0)\n"
"v_mov_b32 v16, 161912\n"
"v_mov_b32 v17, 1\n"
"s_mov_b64 exec, 1\n"
"ds_add_u32 v16, v17\n"
"s_mov_b64 exec, -1\n"
"s_waitcnt vmcnt(10)\n"
"v_mov_b32 v16, 161860\n"
"v_mov_b32 v17, 1\n"
"s_mov_b64 exec, 1\n"
"ds_add_u32 v16, v17\n"
"s_mov_b64 exec, -1\n"
"s_add_u32 s66, s34, 16\n"
"s_mul_i32 s62, s66, 40960\n"
"buffer_load_dwordx4 v[104:107], v53, s[44:47], s62 offen\n"
"buffer_load_dwordx4 v[108:111], v54, s[44:47], s62 offen\n"
"buffer_load_dwordx4 v[112:115], v55, s[44:47], s62 offen\n"
"buffer_load_dwordx4 v[116:119], v56, s[44:47], s62 offen\n"
"buffer_load_dwordx4 v[120:123], v57, s[44:47], s62 offen\n"
"s_mov_b32 s52, s18\n"
"s_and_b32 s53, s19, 0xffff\n"
"s_mov_b32 s54, 1966080\n"
"s_mov_b32 s55, 0x20000\n"
"s_lshl_b32 s60, s34, 6\n"
"s_add_u32 s60, s60, 0\n"
"v_add_u32 v252, s60, v1\n"
"v_mul_u32_u24 v253, 21846, v252\n"
"v_lshrrev_b32 v253, 16, v253\n"
"v_mul_u32_u24 v254, 3, v253\n"
"v_sub_u32 v254, v252, v254\n"
"s_lshr_b32 s60, s6, 5\n"
"s_mul_i32 s60, s60, 80\n"
"v_min_u32 v253, 79, v253\n"
"v_add_u32 v253, s60, v253\n"
"v_mul_u32_u24 v253, 3072, v253\n"
"v_lshlrev_b32 v254, 7, v254\n"
"v_min_u32 v254, 380, v254\n"
"s_mul_i32 s60, s31, 384\n"
"v_add3_u32 v253, v253, v254, s60\n"
"buffer_load_dword v248, v253, s[52:55], 0 offen\n"
"s_setprio 3\n"
"s_waitcnt vmcnt(1)\n"
"v_mov_b32 v16, 161864\n"
"s_mov_b32 s69, 0\n"
"L_sp13:\n"
"ds_read_b32 v17, v16\n"
"s_waitcnt lgkmcnt(0)\n"
"v_cmp_gt_u32 vcc, 4, v17\n"
"s_cbranch_vccz L_spd13\n"
"s_sleep 1\n"
"s_add_u32 s69, s69, 1\n"
"s_cmp_lt_u32 s69, 100000\n"
"s_cbranch_scc1 L_sp13\n"
"L_spd13:\n"
"s_bitcmp1_b32 s31, 0\n"
"s_cbranch_scc1 L_p2ok14\n"
"v_mov_b32 v16, 161856\n"
"s_mov_b32 s69, 0\n"
"L_sp15:\n"
"ds_read_b32 v17, v16\n"
"s_waitcnt lgkmcnt(0)\n"
"v_cmp_gt_u32 vcc, 1, v17\n"
"s_cbranch_vccz L_spd15\n"
"s_sleep 1\n"
"s_add_u32 s69, s69, 1\n"
"s_cmp_lt_u32 s69, 100000\n"
"s_cbranch_scc1 L_sp15\n"
"L_spd15:\n"
"L_p2ok14:\n"
"v_lshlrev_b32 v16, 4, v1\n"
"s_add_u32 s61, s34, 0\n"
"s_mul_i32 s61, s61, 5120\n"
"v_add_u32 v17, s61, v16\n"
"ds_write_b128 v17, v[64:67] offset:0\n"
"ds_write_b128 v17, v[68:71] offset:1024\n"
"ds_write_b128 v17, v[72:75] offset:2048\n"
"ds_write_b128 v17, v[76:79] offset:3072\n"
"ds_write_b128 v17, v[80:83] offset:4096\n"
"s_add_u32 s61, s34, 4\n"
"s_mul_i32 s61, s61, 5120\n"
"v_add_u32 v17, s61, v16\n"
"ds_write_b128 v17, v[84:87] offset:0\n"
"ds_write_b128 v17, v[88:91] offset:1024\n"
"ds_write_b128 v17, v[92:95] offset:2048\n"
"ds_write_b128 v17, v[96:99] offset:3072\n"
"ds_write_b128 v17, v[100:103] offset:4096\n"
"s_waitcnt lgkmcnt(4)\n"
"s_add_u32 s61, s34, 8\n"
"s_mul_i32 s61, s61, 5120\n"
"v_add_u32 v17, s61, v16\n"
"ds_write_b128 v17, v[104:107] offset:0\n"
"ds_write_b128 v17, v[108:111] offset:1024\n"
"ds_write_b128 v17, v[112:115] offset:2048\n"
"ds_write_b128 v17, v[116:119] offset:3072\n"
"ds_write_b128 v17, v[120:123] offset:4096\n"
"v_mov_b32 v16, 161868\n"
"v_mov_b32 v17, 1\n"
"s_mov_b64 exec, 1\n"
"ds_add_u32 v16, v17\n"
"s_mov_b64 exec, -1\n"
"s_mul_i32 s60, s31, 160\n"
"v_add_u32 v17, 0, v1\n"
"v_mul_u32_u24 v18, 6554, v17\n"
"v_lshrrev_b32 v18, 16, v18\n"
"v_mul_u32_u24 v19, 10, v18\n"
"v_sub_u32 v19, v17, v19\n"
"v_mul_u32_u24 v18, 1280, v18\n"
"v_lshl_add_u32 v18, v19, 4, v18\n"
"v_add_u32 v27, s60, v18\n"
"v_add_u32 v17, 64, v1\n"
"v_mul_u32_u24 v18, 6554, v17\n"
"v_lshrrev_b32 v18, 16, v18\n"
"v_mul_u32_u24 v19, 10, v18\n"
"v_sub_u32 v19, v17, v19\n"
"v_mul_u32_u24 v18, 1280, v18\n"
"v_lshl_add_u32 v18, v19, 4, v18\n"
"v_add_u32 v28, s60, v18\n"
"v_add_u32 v17, 128, v1\n"
"v_mul_u32_u24 v18, 6554, v17\n"
"v_lshrrev_b32 v18, 16, v18\n"
"v_mul_u32_u24 v19, 10, v18\n"
"v_sub_u32 v19, v17, v19\n"
"v_mul_u32_u24 v18, 1280, v18\n"
"v_lshl_add_u32 v18, v19, 4, v18\n"
"v_add_u32 v29, s60, v18\n"
"v_add_u32 v17, 192, v1\n"
"v_mul_u32_u24 v18, 6554, v17\n"
"v_lshrrev_b32 v18, 16, v18\n"
"v_mul_u32_u24 v19, 10, v18\n"
"v_sub_u32 v19, v17, v19\n"
"v_mul_u32_u24 v18, 1280, v18\n"
"v_lshl_add_u32 v18, v19, 4, v18\n"
"v_add_u32 v30, s60, v18\n"
"v_add_u32 v17, 256, v1\n"
"v_mul_u32_u24 v18, 6554, v17\n"
"v_lshrrev_b32 v18, 16, v18\n"
"v_mul_u32_u24 v19, 10, v18\n"
"v_sub_u32 v19, v17, v19\n"
"v_mul_u32_u24 v18, 1280, v18\n"
"v_lshl_add_u32 v18, v19, 4, v18\n"
"v_add_u32 v31, s60, v18\n"
"v_bfe_u32 v4, v1, 2, 2\n"
"v_bfe_u32 v6, v1, 4, 1\n"
"v_and_b32 v5, 3, v1\n"
"v_lshlrev_b32 v5, 3, v5\n"
"v_lshl_add_u32 v5, v6, 5, v5\n"
"v_mov_b32 v60, 0\n"
"v_mov_b32 v61, 0\n"
"v_mov_b32 v62, 0\n"
"v_mov_b32 v63, 0\n"
"s_mul_i32 s60, s34, 2624\n"
"v_lshlrev_b32 v6, 4, v1\n"
"v_add_u32 v6, s60, v6\n"
"v_and_b32 v7, 3, v1\n"
"v_lshlrev_b32 v7, 2, v7\n"
"v_add_u32 v7, 161888, v7\n"
"s_lshl_b32 s60, s34, 2\n"
"s_add_u32 s60, s60, 161792\n"
"v_mov_b32 v25, s60\n"
"v_mov_b32 v24, 1\n"
"s_cmp_lg_u32 s34, 0\n"
"s_cbranch_scc1 L_kvw_other\n"
"v_mov_b32 v26, 161916\n"
"s_mov_b32 s69, 0\n"
"L_sp16:\n"
"ds_read_b32 v59, v26\n"
"s_waitcnt lgkmcnt(0)\n"
"v_cmp_gt_u32 vcc, 1, v59\n"
"s_cbranch_vccz L_spd16\n"
"s_sleep 1\n"
"s_add_u32 s69, s69, 1\n"
"s_cmp_lt_u32 s69, 100000\n"
"s_cbranch_scc1 L_sp16\n"
"L_spd16:\n"
"v_lshlrev_b32 v26, 4, v1\n"
"s_and_b32 s60, s6, 31\n"
"s_lshl_b32 s60, s60, 10\n"
"s_add_u32 s60, s60, 262144\n"
"s_mov_b32 s69, 0\n"
"L_gp17:\n"
"buffer_load_dwordx4 v[52:55], v26, s[36:39], s60 offen offset:0 sc1\n"
"s_waitcnt vmcnt(0)\n"
"v_cmp_ne_u32 vcc, s32, v52\n"
"v_cmp_ne_u32 s[66:67], s33, v53\n"
"s_or_b64 vcc, vcc, s[66:67]\n"
"v_cmp_ne_u32 s[66:67], s32, v54\n"
"s_or_b64 vcc, vcc, s[66:67]\n"
"v_cmp_ne_u32 s[66:67], s33, v55\n"
"s_or_b64 vcc, vcc, s[66:67]\n"
"s_cbranch_vccz L_gpd17\n"
"s_sleep 2\n"
"s_add_u32 s69, s69, 1\n"
"s_cmp_lt_u32 s69, 100000\n"
"s_cbranch_scc1 L_gp17\n"
"L_gpd17:\n"
"v_mov_b32 v26, 161852\n"
"v_mov_b32 v59, 1\n"
"s_mov_b64 exec, 1\n"
"ds_add_u32 v26, v59\n"
"s_mov_b64 exec, -1\n"
"s_branch L_kvw_done\n"
"L_kvw_other:\n"
"v_mov_b32 v26, 161852\n"
"s_mov_b32 s69, 0\n"
"L_sp18:\n"
"ds_read_b32 v59, v26\n"
"s_waitcnt lgkmcnt(0)\n"
"v_cmp_gt_u32 vcc, 1, v59\n"
"s_cbranch_vccz L_spd18\n"
"s_sleep 1\n"
"s_add_u32 s69, s69, 1\n"
"s_cmp_lt_u32 s69, 100000\n"
"s_cbranch_scc1 L_sp18\n"
"L_spd18:\n"
"L_kvw_done:\n"
"s_mov_b32 s62, 65536\n"
"buffer_load_dwordx4 v[32:35], v6, s[36:39], s62 offen offset:0 sc1\n"
"buffer_load_dwordx4 v[36:39], v6, s[36:39], s62 offen offset:1024 sc1\n"
"buffer_load_dwordx4 v[40:43], v6, s[36:39], s62 offen offset:1600 sc1\n"
"s_add_u32 s62, s62, 10496\n"
"buffer_load_dwordx4 v[44:47], v6, s[36:39], s62 offen offset:0 sc1\n"
"buffer_load_dwordx4 v[48:51], v6, s[36:39], s62 offen offset:1024 sc1\n"
"buffer_load_dwordx4 v[52:55], v6, s[36:39], s62 offen offset:1600 sc1\n"
"s_add_u32 s62, s62, 10496\n"
"v_mov_b32 v26, 161872\n"
"s_mov_b32 s69, 0\n"
"L_sp19:\n"
"ds_read_b32 v59, v26\n"
"s_waitcnt lgkmcnt(0)\n"
"v_cmp_gt_u32 vcc, 4, v59\n"
"s_cbranch_vccz L_spd19\n"
"s_sleep 1\n"
"s_add_u32 s69, s69, 1\n"
"s_cmp_lt_u32 s69, 100000\n"
"s_cbranch_scc1 L_sp19\n"
"L_spd19:\n"
"s_waitcnt vmcnt(3)\n"
"ds_write_b128 v6, v[32:35] offset:0\n"
"ds_write_b128 v6, v[36:39] offset:1024\n"
"ds_write_b128 v6, v[40:43] offset:1600\n"
"s_mov_b64 exec, 1\n"
"ds_add_u32 v25, v24\n"
"s_mov_b64 exec, -1\n"
"s_waitcnt vmcnt(0)\n"
"ds_write_b128 v6, v[44:47] offset:10496\n"
"ds_write_b128 v6, v[48:51] offset:11520\n"
"ds_write_b128 v6, v[52:55] offset:12096\n"
"s_mov_b64 exec, 1\n"
"ds_add_u32 v25, v24\n"
"s_mov_b64 exec, -1\n"
"s_waitcnt lgkmcnt(0)\n"
"s_mul_i32 s63, s34, 2624\n"
"v_mov_b32 v26, 161880\n"
"s_mov_b32 s69, 0\n"
"L_sp20:\n"
"ds_read_b32 v59, v26\n"
"s_waitcnt lgkmcnt(0)\n"
"v_cmp_gt_u32 vcc, 4, v59\n"
"s_cbranch_vccz L_spd20\n"
"s_sleep 1\n"
"s_add_u32 s69, s69, 1\n"
"s_cmp_lt_u32 s69, 100000\n"
"s_cbranch_scc1 L_sp20\n"
"L_spd20:\n"
"v_mov_b32 v26, 161912\n"
"s_mov_b32 s69, 0\n"
"L_sp21:\n"
"ds_read_b32 v59, v26\n"
"s_waitcnt lgkmcnt(0)\n"
"v_cmp_gt_u32 vcc, 4, v59\n"
"s_cbranch_vccz L_spd21\n"
"s_sleep 1\n"
"s_add_u32 s69, s69, 1\n"
"s_cmp_lt_u32 s69, 100000\n"
"s_cbranch_scc1 L_sp21\n"
"L_spd21:\n"
"s_bitcmp1_b32 s31, 0\n"
"s_cbranch_scc1 L_kvfree\n"
"v_mov_b32 v26, 161856\n"
"s_mov_b32 s69, 0\n"
"L_sp22:\n"
"ds_read_b32 v59, v26\n"
"s_waitcnt lgkmcnt(0)\n"
"v_cmp_gt_u32 vcc, 1, v59\n"
"s_cbranch_vccz L_spd22\n"
"s_sleep 1\n"
"s_add_u32 s69, s69, 1\n"
"s_cmp_lt_u32 s69, 100000\n"
"s_cbranch_scc1 L_sp22\n"
"L_spd22:\n"
"L_kvfree:\n"
"s_and_b32 s64, s34, 1\n"
"s_lshr_b32 s65, s34, 1\n"
"s_mov_b32 s48, s18\n"
"s_and_b32 s49, s19, 0xffff\n"
"s_mov_b32 s50, 1966080\n"
"s_mov_b32 s51, 0x20000\n"
"s_mul_i32 s60, s31, 384\n"
"s_mul_i32 s61, s64, 192\n"
"s_add_u32 s60, s60, s61\n"
"s_mul_i32 s61, s65, 983040\n"
"s_add_u32 s60, s60, s61\n"
"v_add_u32 v11, 0, v1\n"
"v_mul_u32_u24 v12, 5462, v11\n"
"v_lshrrev_b32 v12, 16, v12\n"
"v_mul_u32_u24 v13, 12, v12\n"
"v_sub_u32 v13, v11, v13\n"
"v_mul_u32_u24 v12, 3072, v12\n"
"v_lshl_add_u32 v12, v13, 4, v12\n"
"v_add_u32 v8, s60, v12\n"
"v_add_u32 v11, 64, v1\n"
"v_mul_u32_u24 v12, 5462, v11\n"
"v_lshrrev_b32 v12, 16, v12\n"
"v_mul_u32_u24 v13, 12, v12\n"
"v_sub_u32 v13, v11, v13\n"
"v_mul_u32_u24 v12, 3072, v12\n"
"v_lshl_add_u32 v12, v13, 4, v12\n"
"v_add_u32 v9, s60, v12\n"
"v_add_u32 v11, 128, v1\n"
"v_mul_u32_u24 v12, 5462, v11\n"
"v_lshrrev_b32 v12, 16, v12\n"
"v_mul_u32_u24 v13, 12, v12\n"
"v_sub_u32 v13, v11, v13\n"
"v_mul_u32_u24 v12, 3072, v12\n"
"v_lshl_add_u32 v12, v13, 4, v12\n"
"v_add_u32 v10, s60, v12\n"
"v_lshl_add_u32 v14, v3, 3, v4\n"
"v_mul_u32_u24 v14, 192, v14\n"
"v_add_u32 v14, v14, v5\n"
"v_add_u32 v14, s35, v14\n"
"s_lshl_b32 s60, s65, 5\n"
"v_add_u32 v15, s60, v2\n"
"s_xor_b32 s60, s60, 32\n"
"v_add_u32 v16, s60, v2\n"
"s_mul_i32 s60, s65, 640\n"
"s_add_u32 s60, s60, 78848\n"
"v_mul_u32_u24 v15, 1296, v15\n"
"v_lshl_add_u32 v15, v3, 4, v15\n"
"v_add_u32 v15, s60, v15\n"
"v_mul_u32_u24 v16, 1296, v16\n"
"v_lshl_add_u32 v16, v3, 4, v16\n"
"v_add_u32 v16, s60, v16\n"
"v_mfma_f32_32x32x16_bf16 v[64:79], v[60:63], v[60:63], 0\n"
"v_mfma_f32_32x32x16_bf16 v[80:95], v[60:63], v[60:63], 0\n"
"v_mfma_f32_32x32x16_bf16 v[96:111], v[60:63], v[60:63], 0\n"
"v_mfma_f32_32x32x16_bf16 v[112:127], v[60:63], v[60:63], 0\n"
"v_mfma_f32_32x32x16_bf16 v[128:143], v[60:63], v[60:63], 0\n"
"v_mfma_f32_32x32x16_bf16 v[144:159], v[60:63], v[60:63], 0\n"
"s_mov_b32 s70, 0\n"
"s_add_u32 s61, s35, 0\n"
"s_mov_b32 m0, s61\n"
"s_nop 0\n"
"buffer_load_dwordx4 v8, s[48:51], s70 offen lds\n"
"s_add_u32 s61, s35, 1024\n"
"s_mov_b32 m0, s61\n"
"s_nop 0\n"
"buffer_load_dwordx4 v9, s[48:51], s70 offen lds\n"
"s_add_u32 s61, s35, 2048\n"
"s_mov_b32 m0, s61\n"
"s_nop 0\n"
"buffer_load_dwordx4 v10, s[48:51], s70 offen lds\n"
"s_add_u32 s70, s70, 49152\n"
"s_add_u32 s61, s35, 3072\n"
"s_mov_b32 m0, s61\n"
"s_nop 0\n"
"buffer_load_dwordx4 v8, s[48:51], s70 offen lds\n"
"s_add_u32 s61, s35, 4096\n"
"s_mov_b32 m0, s61\n"
"s_nop 0\n"
"buffer_load_dwordx4 v9, s[48:51], s70 offen lds\n"
"s_add_u32 s61, s35, 5120\n"
"s_mov_b32 m0, s61\n"
"s_nop 0\n"
"buffer_load_dwordx4 v10, s[48:51], s70 offen lds\n"
"s_add_u32 s70, s70, 49152\n"
"s_add_u32 s61, s35, 6144\n"
"s_mov_b32 m0, s61\n"
"s_nop 0\n"
"buffer_load_dwordx4 v8, s[48:51], s70 offen lds\n"
"s_add_u32 s61, s35, 7168\n"
"s_mov_b32 m0, s61\n"
"s_nop 0\n"
"buffer_load_dwordx4 v9, s[48:51], s70 offen lds\n"
"s_add_u32 s61, s35, 8192\n"
"s_mov_b32 m0, s61\n"
"s_nop 0\n"
"buffer_load_dwordx4 v10, s[48:51], s70 offen lds\n"
"s_add_u32 s70, s70, 49152\n"
"s_waitcnt vmcnt(6)\n"
"ds_read_b64_tr_b16 v[160:161], v14 offset:0\n"
"ds_read_b64_tr_b16 v[162:163], v14 offset:768\n"
"ds_read_b64_tr_b16 v[164:165], v14 offset:64\n"
"ds_read_b64_tr_b16 v[166:167], v14 offset:832\n"
"ds_read_b64_tr_b16 v[168:169], v14 offset:128\n"
"ds_read_b64_tr_b16 v[170:171], v14 offset:896\n"
"ds_read_b128 v[184:187], v15 offset:0\n"
"ds_read_b128 v[188:191], v16 offset:0\n"
"s_setprio 3\n"
"s_add_u32 s61, s63, 20992\n"
"s_mov_b32 m0, s61\n"
"s_add_u32 s60, s62, 0\n"
"buffer_load_dwordx4 v6, s[36:39], s60 offen sc1 lds\n"
"s_add_u32 s61, s63, 22016\n"
"s_mov_b32 m0, s61\n"
"s_add_u32 s60, s62, 1024\n"
"buffer_load_dwordx4 v6, s[36:39], s60 offen sc1 lds\n"
"s_add_u32 s61, s63, 22592\n"
"s_mov_b32 m0, s61\n"
"s_add_u32 s60, s62, 1600\n"
"buffer_load_dwordx4 v6, s[36:39], s60 offen sc1 lds\n"
"s_add_u32 s62, s62, 10496\n"
"s_setprio 0\n"
"s_waitcnt lgkmcnt(0)\n"
"s_waitcnt vmcnt(6)\n"
"ds_read_b64_tr_b16 v[172:173], v14 offset:3072\n"
"ds_read_b64_tr_b16 v[174:175], v14 offset:3840\n"
"ds_read_b64_tr_b16 v[176:177], v14 offset:3136\n"
"ds_read_b64_tr_b16 v[178:179], v14 offset:3904\n"
"ds_read_b64_tr_b16 v[180:181], v14 offset:3200\n"
"ds_read_b64_tr_b16 v[182:183], v14 offset:3968\n"
"ds_read_b128 v[192:195], v15 offset:32\n"
"ds_read_b128 v[196:199], v16 offset:32\n"
"s_add_u32 s61, s35, 0\n"
"s_mov_b32 m0, s61\n"
"s_nop 0\n"
"buffer_load_dwordx4 v8, s[48:51], s70 offen lds\n"
"s_add_u32 s61, s35, 1024\n"
"s_mov_b32 m0, s61\n"
"s_nop 0\n"
"buffer_load_dwordx4 v9, s[48:51], s70 offen lds\n"
"s_add_u32 s61, s35, 2048\n"
"s_mov_b32 m0, s61\n"
"s_nop 0\n"
"buffer_load_dwordx4 v10, s[48:51], s70 offen lds\n"
"s_add_u32 s70, s70, 49152\n"
"v_mfma_f32_32x32x16_bf16 v[64:79], v[160:163], v[184:187], v[64:79]\n"
"v_mfma_f32_32x32x16_bf16 v[80:95], v[160:163], v[188:191], v[80:95]\n"
"v_mfma_f32_32x32x16_bf16 v[96:111], v[164:167], v[184:187], v[96:111]\n"
"v_mfma_f32_32x32x16_bf16 v[112:127], v[164:167], v[188:191], v[112:127]\n"
"v_mfma_f32_32x32x16_bf16 v[128:143], v[168:171], v[184:187], v[128:143]\n"
"v_mfma_f32_32x32x16_bf16 v[144:159], v[168:171], v[188:191], v[144:159]\n"
"s_waitcnt vmcnt(3)\n"
"s_mov_b64 exec, 1\n"
"ds_add_u32 v25, v24\n"
"s_mov_b64 exec, -1\n"
"s_waitcnt lgkmcnt(0)\n"
"ds_read_b64_tr_b16 v[160:161], v14 offset:6144\n"
"ds_read_b64_tr_b16 v[162:163], v14 offset:6912\n"
"ds_read_b64_tr_b16 v[164:165], v14 offset:6208\n"
"ds_read_b64_tr_b16 v[166:167], v14 offset:6976\n"
"ds_read_b64_tr_b16 v[168:169], v14 offset:6272\n"
"ds_read_b64_tr_b16 v[170:171], v14 offset:7040\n"
"ds_read_b128 v[184:187], v15 offset:64\n"
"ds_read_b128 v[188:191], v16 offset:64\n"
"s_add_u32 s61, s35, 3072\n"
"s_mov_b32 m0, s61\n"
"s_nop 0\n"
"buffer_load_dwordx4 v8, s[48:51], s70 offen lds\n"
"s_add_u32 s61, s35, 4096\n"
"s_mov_b32 m0, s61\n"
"s_nop 0\n"
"buffer_load_dwordx4 v9, s[48:51], s70 offen lds\n"
"s_add_u32 s61, s35, 5120\n"
"s_mov_b32 m0, s61\n"
"s_nop 0\n"
"buffer_load_dwordx4 v10, s[48:51], s70 offen lds\n"
"s_add_u32 s70, s70, 49152\n"
"v_mfma_f32_32x32x16_bf16 v[64:79], v[172:175], v[192:195], v[64:79]\n"
"v_mfma_f32_32x32x16_bf16 v[80:95], v[172:175], v[196:199], v[80:95]\n"
"v_mfma_f32_32x32x16_bf16 v[96:111], v[176:179], v[192:195], v[96:111]\n"
"v_mfma_f32_32x32x16_bf16 v[112:127], v[176:179], v[196:199], v[112:127]\n"
"v_mfma_f32_32x32x16_bf16 v[128:143], v[180:183], v[192:195], v[128:143]\n"
"v_mfma_f32_32x32x16_bf16 v[144:159], v[180:183], v[196:199], v[144:159]\n"
"s_waitcnt lgkmcnt(0)\n"
"s_waitcnt vmcnt(3)\n"
"ds_read_b64_tr_b16 v[172:173], v14 offset:0\n"
"ds_read_b64_tr_b16 v[174:175], v14 offset:768\n"
"ds_read_b64_tr_b16 v[176:177], v14 offset:64\n"
"ds_read_b64_tr_b16 v[178:179], v14 offset:832\n"
"ds_read_b64_tr_b16 v[180:181], v14 offset:128\n"
"ds_read_b64_tr_b16 v[182:183], v14 offset:896\n"
"ds_read_b128 v[192:195], v15 offset:96\n"
"ds_read_b128 v[196:199], v16 offset:96\n"
"s_add_u32 s61, s35, 6144\n"
"s_mov_b32 m0, s61\n"
"s_nop 0\n"
"buffer_load_dwordx4 v8, s[48:51], s70 offen lds\n"
"s_add_u32 s61, s35, 7168\n"
"s_mov_b32 m0, s61\n"
"s_nop 0\n"
"buffer_load_dwordx4 v9, s[48:51], s70 offen lds\n"
"s_add_u32 s61, s35, 8192\n"
"s_mov_b32 m0, s61\n"
"s_nop 0\n"
"buffer_load_dwordx4 v10, s[48:51], s70 offen lds\n"
"s_add_u32 s70, s70, 49152\n"
"v_mfma_f32_32x32x16_bf16 v[64:79], v[160:163], v[184:187], v[64:79]\n"
"v_mfma_f32_32x32x16_bf16 v[80:95], v[160:163], v[188:191], v[80:95]\n"
"v_mfma_f32_32x32x16_bf16 v[96:111], v[164:167], v[184:187], v[96:111]\n"
"v_mfma_f32_32x32x16_bf16 v[112:127], v[164:167], v[188:191], v[112:127]\n"
"v_mfma_f32_32x32x16_bf16 v[128:143], v[168:171], v[184:187], v[128:143]\n"
"v_mfma_f32_32x32x16_bf16 v[144:159], v[168:171], v[188:191], v[144:159]\n"
"s_waitcnt lgkmcnt(0)\n"
"s_waitcnt vmcnt(3)\n"
"ds_read_b64_tr_b16 v[160:161], v14 offset:3072\n"
"ds_read_b64_tr_b16 v[162:163], v14 offset:3840\n"
"ds_read_b64_tr_b16 v[164:165], v14 offset:3136\n"
"ds_read_b64_tr_b16 v[166:167], v14 offset:3904\n"
"ds_read_b64_tr_b16 v[168:169], v14 offset:3200\n"
"ds_read_b64_tr_b16 v[170:171], v14 offset:3968\n"
"ds_read_b128 v[184:187], v15 offset:128\n"
"ds_read_b128 v[188:191], v16 offset:128\n"
"s_add_u32 s61, s35, 0\n"
"s_mov_b32 m0, s61\n"
"s_nop 0\n"
"buffer_load_dwordx4 v8, s[48:51], s70 offen lds\n"
"s_add_u32 s61, s35, 1024\n"
"s_mov_b32 m0, s61\n"
"s_nop 0\n"
"buffer_load_dwordx4 v9, s[48:51], s70 offen lds\n"
"s_add_u32 s61, s35, 2048\n"
"s_mov_b32 m0, s61\n"
"s_nop 0\n"
"buffer_load_dwordx4 v10, s[48:51], s70 offen lds\n"
"s_add_u32 s70, s70, 49152\n"
"v_mfma_f32_32x32x16_bf16 v[64:79], v[172:175], v[192:195], v[64:79]\n"
"v_mfma_f32_32x32x16_bf16 v[80:95], v[172:175], v[196:199], v[80:95]\n"
"v_mfma_f32_32x32x16_bf16 v[96:111], v[176:179], v[192:195], v[96:111]\n"
"v_mfma_f32_32x32x16_bf16 v[112:127], v[176:179], v[196:199], v[112:127]\n"
"v_mfma_f32_32x32x16_bf16 v[128:143], v[180:183], v[192:195], v[128:143]\n"
"v_mfma_f32_32x32x16_bf16 v[144:159], v[180:183], v[196:199], v[144:159]\n"
"s_setprio 3\n"
"s_add_u32 s61, s63, 31488\n"
"s_mov_b32 m0, s61\n"
"s_add_u32 s60, s62, 0\n"
"buffer_load_dwordx4 v6, s[36:39], s60 offen sc1 lds\n"
"s_add_u32 s61, s63, 32512\n"
"s_mov_b32 m0, s61\n"
"s_add_u32 s60, s62, 1024\n"
"buffer_load_dwordx4 v6, s[36:39], s60 offen sc1 lds\n"
"s_add_u32 s61, s63, 33088\n"
"s_mov_b32 m0, s61\n"
"s_add_u32 s60, s62, 1600\n"
"buffer_load_dwordx4 v6, s[36:39], s60 offen sc1 lds\n"
"s_add_u32 s62, s62, 10496\n"
"s_setprio 0\n"
"s_waitcnt lgkmcnt(0)\n"
"s_waitcnt vmcnt(6)\n"
"ds_read_b64_tr_b16 v[172:173], v14 offset:6144\n"
"ds_read_b64_tr_b16 v[174:175], v14 offset:6912\n"
"ds_read_b64_tr_b16 v[176:177], v14 offset:6208\n"
"ds_read_b64_tr_b16 v[178:179], v14 offset:6976\n"
"ds_read_b64_tr_b16 v[180:181], v14 offset:6272\n"
"ds_read_b64_tr_b16 v[182:183], v14 offset:7040\n"
"ds_read_b128 v[192:195], v15 offset:160\n"
"ds_read_b128 v[196:199], v16 offset:160\n"
"s_add_u32 s61, s35, 3072\n"
"s_mov_b32 m0, s61\n"
"s_nop 0\n"
"buffer_load_dwordx4 v8, s[48:51], s70 offen lds\n"
"s_add_u32 s61, s35, 4096\n"
"s_mov_b32 m0, s61\n"
"s_nop 0\n"
"buffer_load_dwordx4 v9, s[48:51], s70 offen lds\n"
"s_add_u32 s61, s35, 5120\n"
"s_mov_b32 m0, s61\n"
"s_nop 0\n"
"buffer_load_dwordx4 v10, s[48:51], s70 offen lds\n"
"s_add_u32 s70, s70, 49152\n"
"v_mfma_f32_32x32x16_bf16 v[64:79], v[160:163], v[184:187], v[64:79]\n"
"v_mfma_f32_32x32x16_bf16 v[80:95], v[160:163], v[188:191], v[80:95]\n"
"v_mfma_f32_32x32x16_bf16 v[96:111], v[164:167], v[184:187], v[96:111]\n"
"v_mfma_f32_32x32x16_bf16 v[112:127], v[164:167], v[188:191], v[112:127]\n"
"v_mfma_f32_32x32x16_bf16 v[128:143], v[168:171], v[184:187], v[128:143]\n"
"v_mfma_f32_32x32x16_bf16 v[144:159], v[168:171], v[188:191], v[144:159]\n"
"s_waitcnt vmcnt(3)\n"
"s_mov_b64 exec, 1\n"
"ds_add_u32 v25, v24\n"
"s_mov_b64 exec, -1\n"
"s_waitcnt lgkmcnt(0)\n"
"ds_read_b64_tr_b16 v[160:161], v14 offset:0\n"
"ds_read_b64_tr_b16 v[162:163], v14 offset:768\n"
"ds_read_b64_tr_b16 v[164:165], v14 offset:64\n"
"ds_read_b64_tr_b16 v[166:167], v14 offset:832\n"
"ds_read_b64_tr_b16 v[168:169], v14 offset:128\n"
"ds_read_b64_tr_b16 v[170:171], v14 offset:896\n"
"ds_read_b128 v[184:187], v15 offset:192\n"
"ds_read_b128 v[188:191], v16 offset:192\n"
"s_add_u32 s61, s35, 6144\n"
"s_mov_b32 m0, s61\n"
"s_nop 0\n"
"buffer_load_dwordx4 v8, s[48:51], s70 offen lds\n"
"s_add_u32 s61, s35, 7168\n"
"s_mov_b32 m0, s61\n"
"s_nop 0\n"
"buffer_load_dwordx4 v9, s[48:51], s70 offen lds\n"
"s_add_u32 s61, s35, 8192\n"
"s_mov_b32 m0, s61\n"
"s_nop 0\n"
"buffer_load_dwordx4 v10, s[48:51], s70 offen lds\n"
"s_add_u32 s70, s70, 49152\n"
"v_mfma_f32_32x32x16_bf16 v[64:79], v[172:175], v[192:195], v[64:79]\n"
"v_mfma_f32_32x32x16_bf16 v[80:95], v[172:175], v[196:199], v[80:95]\n"
"v_mfma_f32_32x32x16_bf16 v[96:111], v[176:179], v[192:195], v[96:111]\n"
"v_mfma_f32_32x32x16_bf16 v[112:127], v[176:179], v[196:199], v[112:127]\n"
"v_mfma_f32_32x32x16_bf16 v[128:143], v[180:183], v[192:195], v[128:143]\n"
"v_mfma_f32_32x32x16_bf16 v[144:159], v[180:183], v[196:199], v[144:159]\n"
"s_waitcnt lgkmcnt(0)\n"
"s_waitcnt vmcnt(3)\n"
"ds_read_b64_tr_b16 v[172:173], v14 offset:3072\n"
"ds_read_b64_tr_b16 v[174:175], v14 offset:3840\n"
"ds_read_b64_tr_b16 v[176:177], v14 offset:3136\n"
"ds_read_b64_tr_b16 v[178:179], v14 offset:3904\n"
"ds_read_b64_tr_b16 v[180:181], v14 offset:3200\n"
"ds_read_b64_tr_b16 v[182:183], v14 offset:3968\n"
"ds_read_b128 v[192:195], v15 offset:224\n"
"ds_read_b128 v[196:199], v16 offset:224\n"
"s_add_u32 s61, s35, 0\n"
"s_mov_b32 m0, s61\n"
"s_nop 0\n"
"buffer_load_dwordx4 v8, s[48:51], s70 offen lds\n"
"s_add_u32 s61, s35, 1024\n"
"s_mov_b32 m0, s61\n"
"s_nop 0\n"
"buffer_load_dwordx4 v9, s[48:51], s70 offen lds\n"
"s_add_u32 s61, s35, 2048\n"
"s_mov_b32 m0, s61\n"
"s_nop 0\n"
"buffer_load_dwordx4 v10, s[48:51], s70 offen lds\n"
"s_add_u32 s70, s70, 49152\n"
"v_mfma_f32_32x32x16_bf16 v[64:79], v[160:163], v[184:187], v[64:79]\n"
"v_mfma_f32_32x32x16_bf16 v[80:95], v[160:163], v[188:191], v[80:95]\n"
"v_mfma_f32_32x32x16_bf16 v[96:111], v[164:167], v[184:187], v[96:111]\n"
"v_mfma_f32_32x32x16_bf16 v[112:127], v[164:167], v[188:191], v[112:127]\n"
"v_mfma_f32_32x32x16_bf16 v[128:143], v[168:171], v[184:187], v[128:143]\n"
"v_mfma_f32_32x32x16_bf16 v[144:159], v[168:171], v[188:191], v[144:159]\n"
"s_waitcnt lgkmcnt(0)\n"
"s_waitcnt vmcnt(3)\n"
"ds_read_b64_tr_b16 v[160:161], v14 offset:6144\n"
"ds_read_b64_tr_b16 v[162:163], v14 offset:6912\n"
"ds_read_b64_tr_b16 v[164:165], v14 offset:6208\n"
"ds_read_b64_tr_b16 v[166:167], v14 offset:6976\n"
"ds_read_b64_tr_b16 v[168:169], v14 offset:6272\n"
"ds_read_b64_tr_b16 v[170:171], v14 offset:7040\n"
"ds_read_b128 v[184:187], v15 offset:256\n"
"ds_read_b128 v[188:191], v16 offset:256\n"
"s_add_u32 s61, s35, 3072\n"
"s_mov_b32 m0, s61\n"
"s_nop 0\n"
"buffer_load_dwordx4 v8, s[48:51], s70 offen lds\n"
"s_add_u32 s61, s35, 4096\n"
"s_mov_b32 m0, s61\n"
"s_nop 0\n"
"buffer_load_dwordx4 v9, s[48:51], s70 offen lds\n"
"s_add_u32 s61, s35, 5120\n"
"s_mov_b32 m0, s61\n"
"s_nop 0\n"
"buffer_load_dwordx4 v10, s[48:51], s70 offen lds\n"
"s_add_u32 s70, s70, 49152\n"
"v_mfma_f32_32x32x16_bf16 v[64:79], v[172:175], v[192:195], v[64:79]\n"
"v_mfma_f32_32x32x16_bf16 v[80:95], v[172:175], v[196:199], v[80:95]\n"
"v_mfma_f32_32x32x16_bf16 v[96:111], v[176:179], v[192:195], v[96:111]\n"
"v_mfma_f32_32x32x16_bf16 v[112:127], v[176:179], v[196:199], v[112:127]\n"
"v_mfma_f32_32x32x16_bf16 v[128:143], v[180:183], v[192:195], v[128:143]\n"
"v_mfma_f32_32x32x16_bf16 v[144:159], v[180:183], v[196:199], v[144:159]\n"
"s_setprio 3\n"
"s_mov_b32 s69, 0\n"
"L_sp23:\n"
"ds_read_b32 v26, v7\n"
"s_waitcnt lgkmcnt(0)\n"
"v_cmp_gt_u32 vcc, 1, v26\n"
"s_cbranch_vccz L_spd23\n"
"s_sleep 2\n"
"s_add_u32 s69, s69, 1\n"
"s_cmp_lt_u32 s69, 100000\n"
"s_cbranch_scc1 L_sp23\n"
"L_spd23:\n"
"s_setprio 3\n"
"s_add_u32 s61, s63, 0\n"
"s_mov_b32 m0, s61\n"
"s_add_u32 s60, s62, 0\n"
"buffer_load_dwordx4 v6, s[36:39], s60 offen sc1 lds\n"
"s_add_u32 s61, s63, 1024\n"
"s_mov_b32 m0, s61\n"
"s_add_u32 s60, s62, 1024\n"
"buffer_load_dwordx4 v6, s[36:39], s60 offen sc1 lds\n"
"s_add_u32 s61, s63, 1600\n"
"s_mov_b32 m0, s61\n"
"s_add_u32 s60, s62, 1600\n"
"buffer_load_dwordx4 v6, s[36:39], s60 offen sc1 lds\n"
"s_add_u32 s62, s62, 10496\n"
"s_setprio 0\n"
"s_waitcnt vmcnt(6)\n"
"ds_read_b64_tr_b16 v[172:173], v14 offset:0\n"
"ds_read_b64_tr_b16 v[174:175], v14 offset:768\n"
"ds_read_b64_tr_b16 v[176:177], v14 offset:64\n"
"ds_read_b64_tr_b16 v[178:179], v14 offset:832\n"
"ds_read_b64_tr_b16 v[180:181], v14 offset:128\n"
"ds_read_b64_tr_b16 v[182:183], v14 offset:896\n"
"ds_read_b128 v[192:195], v15 offset:288\n"
"ds_read_b128 v[196:199], v16 offset:288\n"
"s_add_u32 s61, s35, 6144\n"
"s_mov_b32 m0, s61\n"
"s_nop 0\n"
"buffer_load_dwordx4 v8, s[48:51], s70 offen lds\n"
"s_add_u32 s61, s35, 7168\n"
"s_mov_b32 m0, s61\n"
"s_nop 0\n"
"buffer_load_dwordx4 v9, s[48:51], s70 offen lds\n"
"s_add_u32 s61, s35, 8192\n"
"s_mov_b32 m0, s61\n"
"s_nop 0\n"
"buffer_load_dwordx4 v10, s[48:51], s70 offen lds\n"
"s_add_u32 s70, s70, 49152\n"
"v_mfma_f32_32x32x16_bf16 v[64:79], v[160:163], v[184:187], v[64:79]\n"
"v_mfma_f32_32x32x16_bf16 v[80:95], v[160:163], v[188:191], v[80:95]\n"
"v_mfma_f32_32x32x16_bf16 v[96:111], v[164:167], v[184:187], v[96:111]\n"
"v_mfma_f32_32x32x16_bf16 v[112:127], v[164:167], v[188:191], v[112:127]\n"
"v_mfma_f32_32x32x16_bf16 v[128:143], v[168:171], v[184:187], v[128:143]\n"
"v_mfma_f32_32x32x16_bf16 v[144:159], v[168:171], v[188:191], v[144:159]\n"
"s_waitcnt vmcnt(3)\n"
"s_mov_b64 exec, 1\n"
"ds_add_u32 v25, v24\n"
"s_mov_b64 exec, -1\n"
"s_waitcnt lgkmcnt(0)\n"
"ds_read_b64_tr_b16 v[160:161], v14 offset:3072\n"
"ds_read_b64_tr_b16 v[162:163], v14 offset:3840\n"
"ds_read_b64_tr_b16 v[164:165], v14 offset:3136\n"
"ds_read_b64_tr_b16 v[166:167], v14 offset:3904\n"
"ds_read_b64_tr_b16 v[168:169], v14 offset:3200\n"
"ds_read_b64_tr_b16 v[170:171], v14 offset:3968\n"
"ds_read_b128 v[184:187], v15 offset:320\n"
"ds_read_b128 v[188:191], v16 offset:320\n"
"s_add_u32 s61, s35, 0\n"
"s_mov_b32 m0, s61\n"
"s_nop 0\n"
"buffer_load_dwordx4 v8, s[48:51], s70 offen lds\n"
"s_add_u32 s61, s35, 1024\n"
"s_mov_b32 m0, s61\n"
"s_nop 0\n"
"buffer_load_dwordx4 v9, s[48:51], s70 offen lds\n"
"s_add_u32 s61, s35, 2048\n"
"s_mov_b32 m0, s61\n"
"s_nop 0\n"
"buffer_load_dwordx4 v10, s[48:51], s70 offen lds\n"
"s_add_u32 s70, s70, 49152\n"
"v_mfma_f32_32x32x16_bf16 v[64:79], v[172:175], v[192:195], v[64:79]\n"
"v_mfma_f32_32x32x16_bf16 v[80:95], v[172:175], v[196:199], v[80:95]\n"
"v_mfma_f32_32x32x16_bf16 v[96:111], v[176:179], v[192:195], v[96:111]\n"
"v_mfma_f32_32x32x16_bf16 v[112:127], v[176:179], v[196:199], v[112:127]\n"
"v_mfma_f32_32x32x16_bf16 v[128:143], v[180:183], v[192:195], v[128:143]\n"
"v_mfma_f32_32x32x16_bf16 v[144:159], v[180:183], v[196:199], v[144:159]\n"
"s_waitcnt lgkmcnt(0)\n"
"s_waitcnt vmcnt(3)\n"
"ds_read_b64_tr_b16 v[172:173], v14 offset:6144\n"
"ds_read_b64_tr_b16 v[174:175], v14 offset:6912\n"
"ds_read_b64_tr_b16 v[176:177], v14 offset:6208\n"
"ds_read_b64_tr_b16 v[178:179], v14 offset:6976\n"
"ds_read_b64_tr_b16 v[180:181], v14 offset:6272\n"
"ds_read_b64_tr_b16 v[182:183], v14 offset:7040\n"
"ds_read_b128 v[192:195], v15 offset:352\n"
"ds_read_b128 v[196:199], v16 offset:352\n"
"s_add_u32 s61, s35, 3072\n"
"s_mov_b32 m0, s61\n"
"s_nop 0\n"
"buffer_load_dwordx4 v8, s[48:51], s70 offen lds\n"
"s_add_u32 s61, s35, 4096\n"
"s_mov_b32 m0, s61\n"
"s_nop 0\n"
"buffer_load_dwordx4 v9, s[48:51], s70 offen lds\n"
"s_add_u32 s61, s35, 5120\n"
"s_mov_b32 m0, s61\n"
"s_nop 0\n"
"buffer_load_dwordx4 v10, s[48:51], s70 offen lds\n"
"s_add_u32 s70, s70, 49152\n"
"v_mfma_f32_32x32x16_bf16 v[64:79], v[160:163], v[184:187], v[64:79]\n"
"v_mfma_f32_32x32x16_bf16 v[80:95], v[160:163], v[188:191], v[80:95]\n"
"v_mfma_f32_32x32x16_bf16 v[96:111], v[164:167], v[184:187], v[96:111]\n"
"v_mfma_f32_32x32x16_bf16 v[112:127], v[164:167], v[188:191], v[112:127]\n"
"v_mfma_f32_32x32x16_bf16 v[128:143], v[168:171], v[184:187], v[128:143]\n"
"v_mfma_f32_32x32x16_bf16 v[144:159], v[168:171], v[188:191], v[144:159]\n"
"s_waitcnt lgkmcnt(0)\n"
"s_waitcnt vmcnt(3)\n"
"ds_read_b64_tr_b16 v[160:161], v14 offset:0\n"
"ds_read_b64_tr_b16 v[162:163], v14 offset:768\n"
"ds_read_b64_tr_b16 v[164:165], v14 offset:64\n"
"ds_read_b64_tr_b16 v[166:167], v14 offset:832\n"
"ds_read_b64_tr_b16 v[168:169], v14 offset:128\n"
"ds_read_b64_tr_b16 v[170:171], v14 offset:896\n"
"ds_read_b128 v[184:187], v15 offset:384\n"
"ds_read_b128 v[188:191], v16 offset:384\n"
"s_add_u32 s61, s35, 6144\n"
"s_mov_b32 m0, s61\n"
"s_nop 0\n"
"buffer_load_dwordx4 v8, s[48:51], s70 offen lds\n"
"s_add_u32 s61, s35, 7168\n"
"s_mov_b32 m0, s61\n"
"s_nop 0\n"
"buffer_load_dwordx4 v9, s[48:51], s70 offen lds\n"
"s_add_u32 s61, s35, 8192\n"
"s_mov_b32 m0, s61\n"
"s_nop 0\n"
"buffer_load_dwordx4 v10, s[48:51], s70 offen lds\n"
"s_add_u32 s70, s70, 49152\n"
"v_mfma_f32_32x32x16_bf16 v[64:79], v[172:175], v[192:195], v[64:79]\n"
"v_mfma_f32_32x32x16_bf16 v[80:95], v[172:175], v[196:199], v[80:95]\n"
"v_mfma_f32_32x32x16_bf16 v[96:111], v[176:179], v[192:195], v[96:111]\n"
"v_mfma_f32_32x32x16_bf16 v[112:127], v[176:179], v[196:199], v[112:127]\n"
"v_mfma_f32_32x32x16_bf16 v[128:143], v[180:183], v[192:195], v[128:143]\n"
"v_mfma_f32_32x32x16_bf16 v[144:159], v[180:183], v[196:199], v[144:159]\n"
"s_setprio 3\n"
"s_mov_b32 s69, 0\n"
"L_sp24:\n"
"ds_read_b32 v26, v7\n"
"s_waitcnt lgkmcnt(0)\n"
"v_cmp_gt_u32 vcc, 2, v26\n"
"s_cbranch_vccz L_spd24\n"
"s_sleep 2\n"
"s_add_u32 s69, s69, 1\n"
"s_cmp_lt_u32 s69, 100000\n"
"s_cbranch_scc1 L_sp24\n"
"L_spd24:\n"
"s_setprio 3\n"
"s_add_u32 s61, s63, 10496\n"
"s_mov_b32 m0, s61\n"
"s_add_u32 s60, s62, 0\n"
"buffer_load_dwordx4 v6, s[36:39], s60 offen sc1 lds\n"
"s_add_u32 s61, s63, 11520\n"
"s_mov_b32 m0, s61\n"
"s_add_u32 s60, s62, 1024\n"
"buffer_load_dwordx4 v6, s[36:39], s60 offen sc1 lds\n"
"s_add_u32 s61, s63, 12096\n"
"s_mov_b32 m0, s61\n"
"s_add_u32 s60, s62, 1600\n"
"buffer_load_dwordx4 v6, s[36:39], s60 offen sc1 lds\n"
"s_add_u32 s62, s62, 10496\n"
"s_setprio 0\n"
"s_waitcnt vmcnt(6)\n"
"ds_read_b64_tr_b16 v[172:173], v14 offset:3072\n"
"ds_read_b64_tr_b16 v[174:175], v14 offset:3840\n"
"ds_read_b64_tr_b16 v[176:177], v14 offset:3136\n"
"ds_read_b64_tr_b16 v[178:179], v14 offset:3904\n"
"ds_read_b64_tr_b16 v[180:181], v14 offset:3200\n"
"ds_read_b64_tr_b16 v[182:183], v14 offset:3968\n"
"ds_read_b128 v[192:195], v15 offset:416\n"
"ds_read_b128 v[196:199], v16 offset:416\n"
"s_add_u32 s61, s35, 0\n"
"s_mov_b32 m0, s61\n"
"s_nop 0\n"
"buffer_load_dwordx4 v8, s[48:51], s70 offen lds\n"
"s_add_u32 s61, s35, 1024\n"
"s_mov_b32 m0, s61\n"
"s_nop 0\n"
"buffer_load_dwordx4 v9, s[48:51], s70 offen lds\n"
"s_add_u32 s61, s35, 2048\n"
"s_mov_b32 m0, s61\n"
"s_nop 0\n"
"buffer_load_dwordx4 v10, s[48:51], s70 offen lds\n"
"s_add_u32 s70, s70, 49152\n"
"v_mfma_f32_32x32x16_bf16 v[64:79], v[160:163], v[184:187], v[64:79]\n"
"v_mfma_f32_32x32x16_bf16 v[80:95], v[160:163], v[188:191], v[80:95]\n"
"v_mfma_f32_32x32x16_bf16 v[96:111], v[164:167], v[184:187], v[96:111]\n"
"v_mfma_f32_32x32x16_bf16 v[112:127], v[164:167], v[188:191], v[112:127]\n"
"v_mfma_f32_32x32x16_bf16 v[128:143], v[168:171], v[184:187], v[128:143]\n"
"v_mfma_f32_32x32x16_bf16 v[144:159], v[168:171], v[188:191], v[144:159]\n"
"s_waitcnt vmcnt(3)\n"
"s_mov_b64 exec, 1\n"
"ds_add_u32 v25, v24\n"
"s_mov_b64 exec, -1\n"
"s_waitcnt lgkmcnt(0)\n"
"ds_read_b64_tr_b16 v[160:161], v14 offset:6144\n"
"ds_read_b64_tr_b16 v[162:163], v14 offset:6912\n"
"ds_read_b64_tr_b16 v[164:165], v14 offset:6208\n"
"ds_read_b64_tr_b16 v[166:167], v14 offset:6976\n"
"ds_read_b64_tr_b16 v[168:169], v14 offset:6272\n"
"ds_read_b64_tr_b16 v[170:171], v14 offset:7040\n"
"ds_read_b128 v[184:187], v15 offset:448\n"
"ds_read_b128 v[188:191], v16 offset:448\n"
"s_add_u32 s61, s35, 3072\n"
"s_mov_b32 m0, s61\n"
"s_nop 0\n"
"buffer_load_dwordx4 v8, s[48:51], s70 offen lds\n"
"s_add_u32 s61, s35, 4096\n"
"s_mov_b32 m0, s61\n"
"s_nop 0\n"
"buffer_load_dwordx4 v9, s[48:51], s70 offen lds\n"
"s_add_u32 s61, s35, 5120\n"
"s_mov_b32 m0, s61\n"
"s_nop 0\n"
"buffer_load_dwordx4 v10, s[48:51], s70 offen lds\n"
"s_add_u32 s70, s70, 49152\n"
"v_mfma_f32_32x32x16_bf16 v[64:79], v[172:175], v[192:195], v[64:79]\n"
"v_mfma_f32_32x32x16_bf16 v[80:95], v[172:175], v[196:199], v[80:95]\n"
"v_mfma_f32_32x32x16_bf16 v[96:111], v[176:179], v[192:195], v[96:111]\n"
"v_mfma_f32_32x32x16_bf16 v[112:127], v[176:179], v[196:199], v[112:127]\n"
"v_mfma_f32_32x32x16_bf16 v[128:143], v[180:183], v[192:195], v[128:143]\n"
"v_mfma_f32_32x32x16_bf16 v[144:159], v[180:183], v[196:199], v[144:159]\n"
"s_waitcnt lgkmcnt(0)\n"
"s_waitcnt vmcnt(3)\n"
"ds_read_b64_tr_b16 v[172:173], v14 offset:0\n"
"ds_read_b64_tr_b16 v[174:175], v14 offset:768\n"
"ds_read_b64_tr_b16 v[176:177], v14 offset:64\n"
"ds_read_b64_tr_b16 v[178:179], v14 offset:832\n"
"ds_read_b64_tr_b16 v[180:181], v14 offset:128\n"
"ds_read_b64_tr_b16 v[182:183], v14 offset:896\n"
"ds_read_b128 v[192:195], v15 offset:480\n"
"ds_read_b128 v[196:199], v16 offset:480\n"
"s_add_u32 s61, s35, 6144\n"
"s_mov_b32 m0, s61\n"
"s_nop 0\n"
"buffer_load_dwordx4 v8, s[48:51], s70 offen lds\n"
"s_add_u32 s61, s35, 7168\n"
"s_mov_b32 m0, s61\n"
"s_nop 0\n"
"buffer_load_dwordx4 v9, s[48:51], s70 offen lds\n"
"s_add_u32 s61, s35, 8192\n"
"s_mov_b32 m0, s61\n"
"s_nop 0\n"
"buffer_load_dwordx4 v10, s[48:51], s70 offen lds\n"
"s_add_u32 s70, s70, 49152\n"
"v_mfma_f32_32x32x16_bf16 v[64:79], v[160:163], v[184:187], v[64:79]\n"
"v_mfma_f32_32x32x16_bf16 v[80:95], v[160:163], v[188:191], v[80:95]\n"
"v_mfma_f32_32x32x16_bf16 v[96:111], v[164:167], v[184:187], v[96:111]\n"
"v_mfma_f32_32x32x16_bf16 v[112:127], v[164:167], v[188:191], v[112:127]\n"
"v_mfma_f32_32x32x16_bf16 v[128:143], v[168:171], v[184:187], v[128:143]\n"
"v_mfma_f32_32x32x16_bf16 v[144:159], v[168:171], v[188:191], v[144:159]\n"
"s_waitcnt lgkmcnt(0)\n"
"s_waitcnt vmcnt(3)\n"
"ds_read_b64_tr_b16 v[160:161], v14 offset:3072\n"
"ds_read_b64_tr_b16 v[162:163], v14 offset:3840\n"
"ds_read_b64_tr_b16 v[164:165], v14 offset:3136\n"
"ds_read_b64_tr_b16 v[166:167], v14 offset:3904\n"
"ds_read_b64_tr_b16 v[168:169], v14 offset:3200\n"
"ds_read_b64_tr_b16 v[170:171], v14 offset:3968\n"
"ds_read_b128 v[184:187], v15 offset:512\n"
"ds_read_b128 v[188:191], v16 offset:512\n"
"s_add_u32 s61, s35, 0\n"
"s_mov_b32 m0, s61\n"
"s_nop 0\n"
"buffer_load_dwordx4 v8, s[48:51], s70 offen lds\n"
"s_add_u32 s61, s35, 1024\n"
"s_mov_b32 m0, s61\n"
"s_nop 0\n"
"buffer_load_dwordx4 v9, s[48:51], s70 offen lds\n"
"s_add_u32 s61, s35, 2048\n"
"s_mov_b32 m0, s61\n"
"s_nop 0\n"
"buffer_load_dwordx4 v10, s[48:51], s70 offen lds\n"
"s_add_u32 s70, s70, 49152\n"
"v_mfma_f32_32x32x16_bf16 v[64:79], v[172:175], v[192:195], v[64:79]\n"
"v_mfma_f32_32x32x16_bf16 v[80:95], v[172:175], v[196:199], v[80:95]\n"
"v_mfma_f32_32x32x16_bf16 v[96:111], v[176:179], v[192:195], v[96:111]\n"
"v_mfma_f32_32x32x16_bf16 v[112:127], v[176:179], v[196:199], v[112:127]\n"
"v_mfma_f32_32x32x16_bf16 v[128:143], v[180:183], v[192:195], v[128:143]\n"
"v_mfma_f32_32x32x16_bf16 v[144:159], v[180:183], v[196:199], v[144:159]\n"
"s_setprio 3\n"
"s_mov_b32 s69, 0\n"
"L_sp25:\n"
"ds_read_b32 v26, v7\n"
"s_waitcnt lgkmcnt(0)\n"
"v_cmp_gt_u32 vcc, 3, v26\n"
"s_cbranch_vccz L_spd25\n"
"s_sleep 2\n"
"s_add_u32 s69, s69, 1\n"
"s_cmp_lt_u32 s69, 100000\n"
"s_cbranch_scc1 L_sp25\n"
"L_spd25:\n"
"s_setprio 3\n"
"s_add_u32 s61, s63, 20992\n"
"s_mov_b32 m0, s61\n"
"s_add_u32 s60, s62, 0\n"
"buffer_load_dwordx4 v6, s[36:39], s60 offen sc1 lds\n"
"s_add_u32 s61, s63, 22016\n"
"s_mov_b32 m0, s61\n"
"s_add_u32 s60, s62, 1024\n"
"buffer_load_dwordx4 v6, s[36:39], s60 offen sc1 lds\n"
"s_add_u32 s61, s63, 22592\n"
"s_mov_b32 m0, s61\n"
"s_add_u32 s60, s62, 1600\n"
"buffer_load_dwordx4 v6, s[36:39], s60 offen sc1 lds\n"
"s_add_u32 s62, s62, 10496\n"
"s_setprio 0\n"
"s_waitcnt vmcnt(6)\n"
"ds_read_b64_tr_b16 v[172:173], v14 offset:6144\n"
"ds_read_b64_tr_b16 v[174:175], v14 offset:6912\n"
"ds_read_b64_tr_b16 v[176:177], v14 offset:6208\n"
"ds_read_b64_tr_b16 v[178:179], v14 offset:6976\n"
"ds_read_b64_tr_b16 v[180:181], v14 offset:6272\n"
"ds_read_b64_tr_b16 v[182:183], v14 offset:7040\n"
"ds_read_b128 v[192:195], v15 offset:544\n"
"ds_read_b128 v[196:199], v16 offset:544\n"
"s_add_u32 s61, s35, 3072\n"
"s_mov_b32 m0, s61\n"
"s_nop 0\n"
"buffer_load_dwordx4 v8, s[48:51], s70 offen lds\n"
"s_add_u32 s61, s35, 4096\n"
"s_mov_b32 m0, s61\n"
"s_nop 0\n"
"buffer_load_dwordx4 v9, s[48:51], s70 offen lds\n"
"s_add_u32 s61, s35, 5120\n"
"s_mov_b32 m0, s61\n"
"s_nop 0\n"
"buffer_load_dwordx4 v10, s[48:51], s70 offen lds\n"
"s_add_u32 s70, s70, 49152\n"
"v_mfma_f32_32x32x16_bf16 v[64:79], v[160:163], v[184:187], v[64:79]\n"
"v_mfma_f32_32x32x16_bf16 v[80:95], v[160:163], v[188:191], v[80:95]\n"
"v_mfma_f32_32x32x16_bf16 v[96:111], v[164:167], v[184:187], v[96:111]\n"
"v_mfma_f32_32x32x16_bf16 v[112:127], v[164:167], v[188:191], v[112:127]\n"
"v_mfma_f32_32x32x16_bf16 v[128:143], v[168:171], v[184:187], v[128:143]\n"
"v_mfma_f32_32x32x16_bf16 v[144:159], v[168:171], v[188:191], v[144:159]\n"
"s_waitcnt vmcnt(3)\n"
"s_mov_b64 exec, 1\n"
"ds_add_u32 v25, v24\n"
"s_mov_b64 exec, -1\n"
"s_waitcnt lgkmcnt(0)\n"
"ds_read_b64_tr_b16 v[160:161], v14 offset:0\n"
"ds_read_b64_tr_b16 v[162:163], v14 offset:768\n"
"ds_read_b64_tr_b16 v[164:165], v14 offset:64\n"
"ds_read_b64_tr_b16 v[166:167], v14 offset:832\n"
"ds_read_b64_tr_b16 v[168:169], v14 offset:128\n"
"ds_read_b64_tr_b16 v[170:171], v14 offset:896\n"
"ds_read_b128 v[184:187], v15 offset:576\n"
"ds_read_b128 v[188:191], v16 offset:576\n"
"v_mfma_f32_32x32x16_bf16 v[64:79], v[172:175], v[192:195], v[64:79]\n"
"v_mfma_f32_32x32x16_bf16 v[80:95], v[172:175], v[196:199], v[80:95]\n"
"v_mfma_f32_32x32x16_bf16 v[96:111], v[176:179], v[192:195], v[96:111]\n"
"v_mfma_f32_32x32x16_bf16 v[112:127], v[176:179], v[196:199], v[112:127]\n"
"v_mfma_f32_32x32x16_bf16 v[128:143], v[180:183], v[192:195], v[128:143]\n"
"v_mfma_f32_32x32x16_bf16 v[144:159], v[180:183], v[196:199], v[144:159]\n"
"s_waitcnt lgkmcnt(0)\n"
"s_waitcnt vmcnt(0)\n"
"ds_read_b64_tr_b16 v[172:173], v14 offset:3072\n"
"ds_read_b64_tr_b16 v[174:175], v14 offset:3840\n"
"ds_read_b64_tr_b16 v[176:177], v14 offset:3136\n"
"ds_read_b64_tr_b16 v[178:179], v14 offset:3904\n"
"ds_read_b64_tr_b16 v[180:181], v14 offset:3200\n"
"ds_read_b64_tr_b16 v[182:183], v14 offset:3968\n"
"ds_read_b128 v[192:195], v15 offset:608\n"
"ds_read_b128 v[196:199], v16 offset:608\n"
"v_mfma_f32_32x32x16_bf16 v[64:79], v[160:163], v[184:187], v[64:79]\n"
"v_mfma_f32_32x32x16_bf16 v[80:95], v[160:163], v[188:191], v[80:95]\n"
"v_mfma_f32_32x32x16_bf16 v[96:111], v[164:167], v[184:187], v[96:111]\n"
"v_mfma_f32_32x32x16_bf16 v[112:127], v[164:167], v[188:191], v[112:127]\n"
"v_mfma_f32_32x32x16_bf16 v[128:143], v[168:171], v[184:187], v[128:143]\n"
"v_mfma_f32_32x32x16_bf16 v[144:159], v[168:171], v[188:191], v[144:159]\n"
"s_waitcnt lgkmcnt(0)\n"
"v_mfma_f32_32x32x16_bf16 v[64:79], v[172:175], v[192:195], v[64:79]\n"
"v_mfma_f32_32x32x16_bf16 v[80:95], v[172:175], v[196:199], v[80:95]\n"
"v_mfma_f32_32x32x16_bf16 v[96:111], v[176:179], v[192:195], v[96:111]\n"
"v_mfma_f32_32x32x16_bf16 v[112:127], v[176:179], v[196:199], v[112:127]\n"
"v_mfma_f32_32x32x16_bf16 v[128:143], v[180:183], v[192:195], v[128:143]\n"
"v_mfma_f32_32x32x16_bf16 v[144:159], v[180:183], v[196:199], v[144:159]\n"
"s_setprio 3\n"
"s_mov_b32 s69, 0\n"
"L_sp26:\n"
"ds_read_b32 v26, v7\n"
"s_waitcnt lgkmcnt(0)\n"
"v_cmp_gt_u32 vcc, 4, v26\n"
"s_cbranch_vccz L_spd26\n"
"s_sleep 2\n"
"s_add_u32 s69, s69, 1\n"
"s_cmp_lt_u32 s69, 100000\n"
"s_cbranch_scc1 L_sp26\n"
"L_spd26:\n"
"s_setprio 3\n"
"s_add_u32 s61, s63, 31488\n"
"s_mov_b32 m0, s61\n"
"s_add_u32 s60, s62, 0\n"
"buffer_load_dwordx4 v6, s[36:39], s60 offen sc1 lds\n"
"s_add_u32 s61, s63, 32512\n"
"s_mov_b32 m0, s61\n"
"s_add_u32 s60, s62, 1024\n"
"buffer_load_dwordx4 v6, s[36:39], s60 offen sc1 lds\n"
"s_add_u32 s61, s63, 33088\n"
"s_mov_b32 m0, s61\n"
"s_add_u32 s60, s62, 1600\n"
"buffer_load_dwordx4 v6, s[36:39], s60 offen sc1 lds\n"
"s_add_u32 s62, s62, 10496\n"
"s_waitcnt lgkmcnt(0)\n"
"s_mov_b32 s52, s22\n"
"s_and_b32 s53, s23, 0xffff\n"
"s_mov_b32 s54, 1966080\n"
"s_mov_b32 s55, 0x20000\n"
"s_lshl_b32 s60, s34, 6\n"
"s_add_u32 s60, s60, 0\n"
"v_add_u32 v252, s60, v1\n"
"v_lshrrev_b32 v253, 1, v252\n"
"v_and_b32 v254, 1, v252\n"
"s_lshr_b32 s60, s6, 5\n"
"s_mul_i32 s60, s60, 192\n"
"v_min_u32 v253, 191, v253\n"
"v_add_u32 v253, s60, v253\n"
"v_mul_u32_u24 v253, 1280, v253\n"
"v_lshlrev_b32 v254, 7, v254\n"
"v_min_u32 v254, 156, v254\n"
"s_mul_i32 s60, s31, 160\n"
"v_add3_u32 v253, v253, v254, s60\n"
"buffer_load_dword v249, v253, s[52:55], 0 offen\n"
"s_lshl_b32 s60, s34, 6\n"
"s_add_u32 s60, s60, 256\n"
"v_add_u32 v252, s60, v1\n"
"v_lshrrev_b32 v253, 1, v252\n"
"v_and_b32 v254, 1, v252\n"
"s_lshr_b32 s60, s6, 5\n"
"s_mul_i32 s60, s60, 192\n"
"v_min_u32 v253, 191, v253\n"
"v_add_u32 v253, s60, v253\n"
"v_mul_u32_u24 v253, 1280, v253\n"
"v_lshlrev_b32 v254, 7, v254\n"
"v_min_u32 v254, 156, v254\n"
"s_mul_i32 s60, s31, 160\n"
"v_add3_u32 v253, v253, v254, s60\n"
"buffer_load_dword v250, v253, s[52:55], 0 offen\n"
"s_mov_b32 s52, s16\n"
"s_and_b32 s53, s17, 0xffff\n"
"s_mov_b32 s54, 819200\n"
"s_mov_b32 s55, 0x20000\n"
"s_lshl_b32 s60, s34, 6\n"
"s_add_u32 s60, s60, 0\n"
"v_add_u32 v252, s60, v1\n"
"v_lshrrev_b32 v253, 1, v252\n"
"v_and_b32 v254, 1, v252\n"
"s_lshr_b32 s60, s6, 5\n"
"s_mul_i32 s60, s60, 80\n"
"v_min_u32 v253, 79, v253\n"
"v_add_u32 v253, s60, v253\n"
"v_mul_u32_u24 v253, 1280, v253\n"
"v_lshlrev_b32 v254, 7, v254\n"
"v_min_u32 v254, 156, v254\n"
"s_mul_i32 s60, s31, 160\n"
"v_add3_u32 v253, v253, v254, s60\n"
"buffer_load_dword v251, v253, s[52:55], 0 offen\n"
"s_mov_b32 s52, s20\n"
"s_and_b32 s53, s21, 0xffff\n"
"s_mov_b32 s54, 3072\n"
"s_mov_b32 s55, 0x20000\n"
"s_mul_i32 s60, s31, 384\n"
"s_mul_i32 s61, s64, 192\n"
"s_add_u32 s60, s60, s61\n"
"v_lshlrev_b32 v17, 1, v2\n"
"buffer_load_ushort v160, v17, s[52:55], s60 offen offset:0\n"
"v_mov_b32 v161, 0\n"
"v_mov_b32 v162, 0\n"
"v_mov_b32 v163, 0\n"
"buffer_load_ushort v164, v17, s[52:55], s60 offen offset:64\n"
"v_mov_b32 v165, 0\n"
"v_mov_b32 v166, 0\n"
"v_mov_b32 v167, 0\n"
"buffer_load_ushort v168, v17, s[52:55], s60 offen offset:128\n"
"v_mov_b32 v169, 0\n"
"v_mov_b32 v170, 0\n"
"v_mov_b32 v171, 0\n"
"v_lshlrev_b32 v18, 4, v1\n"
"v_mov_b32 v20, 161920\n"
"v_mov_b32 v21, 1\n"
"s_mov_b64 exec, 1\n"
"ds_add_u32 v20, v21\n"
"s_mov_b64 exec, -1\n"
"v_mov_b32 v20, 161920\n"
"s_mov_b32 s69, 0\n"
"L_sp27:\n"
"ds_read_b32 v21, v20\n"
"s_waitcnt lgkmcnt(0)\n"
"v_cmp_gt_u32 vcc, 4, v21\n"
"s_cbranch_vccz L_spd27\n"
"s_sleep 1\n"
"s_add_u32 s69, s69, 1\n"
"s_cmp_lt_u32 s69, 100000\n"
"s_cbranch_scc1 L_sp27\n"
"L_spd27:\n"
"v_mov_b32 v20, 161848\n"
"s_mov_b32 s69, 0\n"
"L_sp28:\n"
"ds_read_b32 v21, v20\n"
"s_waitcnt lgkmcnt(0)\n"
"v_cmp_gt_u32 vcc, 4, v21\n"
"s_cbranch_vccz L_spd28\n"
"s_sleep 1\n"
"s_add_u32 s69, s69, 1\n"
"s_cmp_lt_u32 s69, 100000\n"
"s_cbranch_scc1 L_sp28\n"
"L_spd28:\n"
"s_mul_i32 s61, s34, 12288\n"
"s_add_u32 s61, s61, 78848\n"
"v_add_u32 v19, s61, v18\n"
"ds_write_b128 v19, v[80:83] offset:0\n"
"ds_write_b128 v19, v[84:87] offset:1024\n"
"ds_write_b128 v19, v[88:91] offset:2048\n"
"ds_write_b128 v19, v[92:95] offset:3072\n"
"ds_write_b128 v19, v[112:115] offset:4096\n"
"ds_write_b128 v19, v[116:119] offset:5120\n"
"ds_write_b128 v19, v[120:123] offset:6144\n"
"ds_write_b128 v19, v[124:127] offset:7168\n"
"ds_write_b128 v19, v[144:147] offset:8192\n"
"ds_write_b128 v19, v[148:151] offset:9216\n"
"ds_write_b128 v19, v[152:155] offset:10240\n"
"ds_write_b128 v19, v[156:159] offset:11264\n"
"s_add_u32 s60, s34, 8\n"
"s_lshl_b32 s60, s60, 2\n"
"s_add_u32 s60, s60, 161792\n"
"v_mov_b32 v20, s60\n"
"v_mov_b32 v21, 1\n"
"s_mov_b64 exec, 1\n"
"ds_write_b32 v20, v21\n"
"s_mov_b64 exec, -1\n"
"s_xor_b32 s61, s34, 2\n"
"s_add_u32 s60, s61, 8\n"
"s_lshl_b32 s60, s60, 2\n"
"s_add_u32 s60, s60, 161792\n"
"v_mov_b32 v20, s60\n"
"s_waitcnt vmcnt(6)\n"
"s_mov_b64 exec, 1\n"
"ds_add_u32 v25, v24\n"
"s_mov_b64 exec, -1\n"
"s_mov_b32 s69, 0\n"
"L_sp29:\n"
"ds_read_b32 v21, v20\n"
"s_waitcnt lgkmcnt(0)\n"
"v_cmp_gt_u32 vcc, 1, v21\n"
"s_cbranch_vccz L_spd29\n"
"s_sleep 1\n"
"s_add_u32 s69, s69, 1\n"
"s_cmp_lt_u32 s69, 100000\n"
"s_cbranch_scc1 L_sp29\n"
"L_spd29:\n"
"s_mul_i32 s61, s61, 12288\n"
"s_add_u32 s61, s61, 78848\n"
"v_add_u32 v19, s61, v18\n"
"ds_read_b128 v[80:83], v19 offset:0\n"
"ds_read_b128 v[84:87], v19 offset:1024\n"
"ds_read_b128 v[88:91], v19 offset:2048\n"
"ds_read_b128 v[92:95], v19 offset:3072\n"
"ds_read_b128 v[112:115], v19 offset:4096\n"
"ds_read_b128 v[116:119], v19 offset:5120\n"
"ds_read_b128 v[120:123], v19 offset:6144\n"
"ds_read_b128 v[124:127], v19 offset:7168\n"
"ds_read_b128 v[144:147], v19 offset:8192\n"
"ds_read_b128 v[148:151], v19 offset:9216\n"
"ds_read_b128 v[152:155], v19 offset:10240\n"
"ds_read_b128 v[156:159], v19 offset:11264\n"
"v_mov_b32 v184, 0x3f80\n"
"v_mov_b32 v185, 0\n"
"v_mov_b32 v186, 0\n"
"v_mov_b32 v187, 0\n"
"s_mov_b32 exec_lo, 0\n"
"v_mov_b32 v184, 0\n"
"s_mov_b64 exec, -1\n"
"s_waitcnt lgkmcnt(0)\n"
"s_waitcnt vmcnt(0)\n"
"v_add_f32 v64, v64, v80\n"
"v_add_f32 v65, v65, v81\n"
"v_add_f32 v66, v66, v82\n"
"v_add_f32 v67, v67, v83\n"
"v_add_f32 v68, v68, v84\n"
"v_add_f32 v69, v69, v85\n"
"v_add_f32 v70, v70, v86\n"
"v_add_f32 v71, v71, v87\n"
"v_add_f32 v72, v72, v88\n"
"v_add_f32 v73, v73, v89\n"
"v_add_f32 v74, v74, v90\n"
"v_add_f32 v75, v75, v91\n"
"v_add_f32 v76, v76, v92\n"
"v_add_f32 v77, v77, v93\n"
"v_add_f32 v78, v78, v94\n"
"v_add_f32 v79, v79, v95\n"
"v_add_f32 v96, v96, v112\n"
"v_add_f32 v97, v97, v113\n"
"v_add_f32 v98, v98, v114\n"
"v_add_f32 v99, v99, v115\n"
"v_add_f32 v100, v100, v116\n"
"v_add_f32 v101, v101, v117\n"
"v_add_f32 v102, v102, v118\n"
"v_add_f32 v103, v103, v119\n"
"v_add_f32 v104, v104, v120\n"
"v_add_f32 v105, v105, v121\n"
"v_add_f32 v106, v106, v122\n"
"v_add_f32 v107, v107, v123\n"
"v_add_f32 v108, v108, v124\n"
"v_add_f32 v109, v109, v125\n"
"v_add_f32 v110, v110, v126\n"
"v_add_f32 v111, v111, v127\n"
"v_add_f32 v128, v128, v144\n"
"v_add_f32 v129, v129, v145\n"
"v_add_f32 v130, v130, v146\n"
"v_add_f32 v131, v131, v147\n"
"v_add_f32 v132, v132, v148\n"
"v_add_f32 v133, v133, v149\n"
"v_add_f32 v134, v134, v150\n"
"v_add_f32 v135, v135, v151\n"
"v_add_f32 v136, v136, v152\n"
"v_add_f32 v137, v137, v153\n"
"v_add_f32 v138, v138, v154\n"
"v_add_f32 v139, v139, v155\n"
"v_add_f32 v140, v140, v156\n"
"v_add_f32 v141, v141, v157\n"
"v_add_f32 v142, v142, v158\n"
"v_add_f32 v143, v143, v159\n"
"v_mfma_f32_32x32x16_bf16 v[64:79], v[160:163], v[184:187], v[64:79]\n"
"v_mfma_f32_32x32x16_bf16 v[96:111], v[164:167], v[184:187], v[96:111]\n"
"v_mfma_f32_32x32x16_bf16 v[128:143], v[168:171], v[184:187], v[128:143]\n"
"s_setprio 3\n"
"s_mov_b32 s69, 0\n"
"L_sp30:\n"
"ds_read_b32 v26, v7\n"
"s_waitcnt lgkmcnt(0)\n"
"v_cmp_gt_u32 vcc, 5, v26\n"
"s_cbranch_vccz L_spd30\n"
"s_sleep 2\n"
"s_add_u32 s69, s69, 1\n"
"s_cmp_lt_u32 s69, 100000\n"
"s_cbranch_scc1 L_sp30\n"
"L_spd30:\n"
"s_setprio 3\n"
"s_add_u32 s61, s63, 0\n"
"s_mov_b32 m0, s61\n"
"s_add_u32 s60, s62, 0\n"
"buffer_load_dwordx4 v6, s[36:39], s60 offen sc1 lds\n"
"s_add_u32 s61, s63, 1024\n"
"s_mov_b32 m0, s61\n"
"s_add_u32 s60, s62, 1024\n"
"buffer_load_dwordx4 v6, s[36:39], s60 offen sc1 lds\n"
"s_add_u32 s61, s63, 1600\n"
"s_mov_b32 m0, s61\n"
"s_add_u32 s60, s62, 1600\n"
"buffer_load_dwordx4 v6, s[36:39], s60 offen sc1 lds\n"
"s_add_u32 s62, s62, 10496\n"
"s_setprio 3\n"
"s_mul_i32 s60, s30, 96\n"
"s_mul_i32 s61, s31, 12\n"
"s_add_u32 s60, s60, s61\n"
"s_mul_i32 s61, s64, 6\n"
"s_add_u32 s60, s60, s61\n"
"s_lshl_b32 s60, s60, 11\n"
"s_lshl_b32 s61, s65, 10\n"
"s_add_u32 s60, s60, s61\n"
"s_add_u32 s66, s60, 3145728\n"
"v_mov_b32 v56, -1\n"
"v_mov_b32 v57, -1\n"
"v_mov_b32 v58, -1\n"
"v_mov_b32 v59, -1\n"
"s_lshl_b32 s61, s30, 3\n"
"s_add_u32 s61, s61, s31\n"
"s_mul_i32 s61, s61, 10240\n"
"s_add_u32 s67, s61, 524288\n"
"s_add_u32 s61, s34, 0\n"
"s_min_u32 s61, s61, 9\n"
"s_lshl_b32 s61, s61, 10\n"
"s_add_u32 s61, s61, s67\n"
"buffer_store_dwordx4 v[56:59], v18, s[36:39], s61 offen sc1\n"
"s_add_u32 s61, s34, 4\n"
"s_min_u32 s61, s61, 9\n"
"s_lshl_b32 s61, s61, 10\n"
"s_add_u32 s61, s61, s67\n"
"buffer_store_dwordx4 v[56:59], v18, s[36:39], s61 offen sc1\n"
"s_add_u32 s61, s34, 8\n"
"s_min_u32 s61, s61, 9\n"
"s_lshl_b32 s61, s61, 10\n"
"s_add_u32 s61, s61, s67\n"
"buffer_store_dwordx4 v[56:59], v18, s[36:39], s61 offen sc1\n"
"v_max_f32 v64, 0, v64\n"
"v_max_f32 v65, 0, v65\n"
"v_max_f32 v66, 0, v66\n"
"v_max_f32 v67, 0, v67\n"
"v_max_f32 v68, 0, v68\n"
"v_max_f32 v69, 0, v69\n"
"v_max_f32 v70, 0, v70\n"
"v_max_f32 v71, 0, v71\n"
"v_max_f32 v72, 0, v72\n"
"v_max_f32 v73, 0, v73\n"
"v_max_f32 v74, 0, v74\n"
"v_max_f32 v75, 0, v75\n"
"v_max_f32 v76, 0, v76\n"
"v_max_f32 v77, 0, v77\n"
"v_max_f32 v78, 0, v78\n"
"v_max_f32 v79, 0, v79\n"
"v_pk_mul_f32 v[64:65], v[64:65], v[64:65]\n"
"v_pk_mul_f32 v[66:67], v[66:67], v[66:67]\n"
"v_pk_mul_f32 v[68:69], v[68:69], v[68:69]\n"
"v_pk_mul_f32 v[70:71], v[70:71], v[70:71]\n"
"v_pk_mul_f32 v[72:73], v[72:73], v[72:73]\n"
"v_pk_mul_f32 v[74:75], v[74:75], v[74:75]\n"
"v_pk_mul_f32 v[76:77], v[76:77], v[76:77]\n"
"v_pk_mul_f32 v[78:79], v[78:79], v[78:79]\n"
"v_cvt_pk_bf16_f32 v188, v64, v65\n"
"v_cvt_pk_bf16_f32 v189, v66, v67\n"
"v_cvt_pk_bf16_f32 v190, v68, v69\n"
"v_cvt_pk_bf16_f32 v191, v70, v71\n"
"v_cvt_pk_bf16_f32 v192, v72, v73\n"
"v_cvt_pk_bf16_f32 v193, v74, v75\n"
"v_cvt_pk_bf16_f32 v194, v76, v77\n"
"v_cvt_pk_bf16_f32 v195, v78, v79\n"
"s_waitcnt vmcnt(3)\n"
"s_mov_b64 exec, 1\n"
"ds_add_u32 v25, v24\n"
"s_mov_b64 exec, -1\n"
"s_add_u32 s61, s66, 0\n"
"buffer_store_dwordx4 v[188:191], v18, s[36:39], s61 offen sc1\n"
"s_add_u32 s61, s66, 2048\n"
"buffer_store_dwordx4 v[192:195], v18, s[36:39], s61 offen sc1\n"
"v_max_f32 v96, 0, v96\n"
"v_max_f32 v97, 0, v97\n"
"v_max_f32 v98, 0, v98\n"
"v_max_f32 v99, 0, v99\n"
"v_max_f32 v100, 0, v100\n"
"v_max_f32 v101, 0, v101\n"
"v_max_f32 v102, 0, v102\n"
"v_max_f32 v103, 0, v103\n"
"v_max_f32 v104, 0, v104\n"
"v_max_f32 v105, 0, v105\n"
"v_max_f32 v106, 0, v106\n"
"v_max_f32 v107, 0, v107\n"
"v_max_f32 v108, 0, v108\n"
"v_max_f32 v109, 0, v109\n"
"v_max_f32 v110, 0, v110\n"
"v_max_f32 v111, 0, v111\n"
"v_pk_mul_f32 v[96:97], v[96:97], v[96:97]\n"
"v_pk_mul_f32 v[98:99], v[98:99], v[98:99]\n"
"v_pk_mul_f32 v[100:101], v[100:101], v[100:101]\n"
"v_pk_mul_f32 v[102:103], v[102:103], v[102:103]\n"
"v_pk_mul_f32 v[104:105], v[104:105], v[104:105]\n"
"v_pk_mul_f32 v[106:107], v[106:107], v[106:107]\n"
"v_pk_mul_f32 v[108:109], v[108:109], v[108:109]\n"
"v_pk_mul_f32 v[110:111], v[110:111], v[110:111]\n"
"v_cvt_pk_bf16_f32 v196, v96, v97\n"
"v_cvt_pk_bf16_f32 v197, v98, v99\n"
"v_cvt_pk_bf16_f32 v198, v100, v101\n"
"v_cvt_pk_bf16_f32 v199, v102, v103\n"
"v_cvt_pk_bf16_f32 v200, v104, v105\n"
"v_cvt_pk_bf16_f32 v201, v106, v107\n"
"v_cvt_pk_bf16_f32 v202, v108, v109\n"
"v_cvt_pk_bf16_f32 v203, v110, v111\n"
"s_add_u32 s61, s66, 4096\n"
"buffer_store_dwordx4 v[196:199], v18, s[36:39], s61 offen sc1\n"
"s_add_u32 s61, s66, 6144\n"
"buffer_store_dwordx4 v[200:203], v18, s[36:39], s61 offen sc1\n"
"v_max_f32 v128, 0, v128\n"
"v_max_f32 v129, 0, v129\n"
"v_max_f32 v130, 0, v130\n"
"v_max_f32 v131, 0, v131\n"
"v_max_f32 v132, 0, v132\n"
"v_max_f32 v133, 0, v133\n"
"v_max_f32 v134, 0, v134\n"
"v_max_f32 v135, 0, v135\n"
"v_max_f32 v136, 0, v136\n"
"v_max_f32 v137, 0, v137\n"
"v_max_f32 v138, 0, v138\n"
"v_max_f32 v139, 0, v139\n"
"v_max_f32 v140, 0, v140\n"
"v_max_f32 v141, 0, v141\n"
"v_max_f32 v142, 0, v142\n"
"v_max_f32 v143, 0, v143\n"
"v_pk_mul_f32 v[128:129], v[128:129], v[128:129]\n"
"v_pk_mul_f32 v[130:131], v[130:131], v[130:131]\n"
"v_pk_mul_f32 v[132:133], v[132:133], v[132:133]\n"
"v_pk_mul_f32 v[134:135], v[134:135], v[134:135]\n"
"v_pk_mul_f32 v[136:137], v[136:137], v[136:137]\n"
"v_pk_mul_f32 v[138:139], v[138:139], v[138:139]\n"
"v_pk_mul_f32 v[140:141], v[140:141], v[140:141]\n"
"v_pk_mul_f32 v[142:143], v[142:143], v[142:143]\n"
"v_cvt_pk_bf16_f32 v204, v128, v129\n"
"v_cvt_pk_bf16_f32 v205, v130, v131\n"
"v_cvt_pk_bf16_f32 v206, v132, v133\n"
"v_cvt_pk_bf16_f32 v207, v134, v135\n"
"v_cvt_pk_bf16_f32 v208, v136, v137\n"
"v_cvt_pk_bf16_f32 v209, v138, v139\n"
"v_cvt_pk_bf16_f32 v210, v140, v141\n"
"v_cvt_pk_bf16_f32 v211, v142, v143\n"
"s_add_u32 s61, s66, 8192\n"
"buffer_store_dwordx4 v[204:207], v18, s[36:39], s61 offen sc1\n"
"s_add_u32 s61, s66, 10240\n"
"buffer_store_dwordx4 v[208:211], v18, s[36:39], s61 offen sc1\n"
"s_setprio 3\n"
"s_mov_b32 s69, 0\n"
"L_sp31:\n"
"ds_read_b32 v26, v7\n"
"s_waitcnt lgkmcnt(0)\n"
"v_cmp_gt_u32 vcc, 6, v26\n"
"s_cbranch_vccz L_spd31\n"
"s_sleep 2\n"
"s_add_u32 s69, s69, 1\n"
"s_cmp_lt_u32 s69, 100000\n"
"s_cbranch_scc1 L_sp31\n"
"L_spd31:\n"
"s_setprio 3\n"
"s_add_u32 s61, s63, 10496\n"
"s_mov_b32 m0, s61\n"
"s_add_u32 s60, s62, 0\n"
"buffer_load_dwordx4 v6, s[36:39], s60 offen sc1 lds\n"
"s_add_u32 s61, s63, 11520\n"
"s_mov_b32 m0, s61\n"
"s_add_u32 s60, s62, 1024\n"
"buffer_load_dwordx4 v6, s[36:39], s60 offen sc1 lds\n"
"s_add_u32 s61, s63, 12096\n"
"s_mov_b32 m0, s61\n"
"s_add_u32 s60, s62, 1600\n"
"buffer_load_dwordx4 v6, s[36:39], s60 offen sc1 lds\n"
"s_add_u32 s62, s62, 10496\n"
"s_waitcnt vmcnt(3)\n"
"v_mov_b32 v20, 161840\n"
"v_mov_b32 v21, 1\n"
"s_mov_b64 exec, 1\n"
"ds_add_rtn_u32 v22, v20, v21\n"
"s_waitcnt lgkmcnt(0)\n"
"v_readfirstlane_b32 s60, v22\n"
"s_mov_b64 exec, -1\n"
"s_cmp_lg_u32 s60, 3\n"
"s_cbranch_scc1 L_hnoflag\n"
"s_lshl_b32 s60, s30, 6\n"
"s_add_u32 s60, s60, s31\n"
"s_lshl_b32 s60, s60, 3\n"
"s_add_u32 s60, s60, 4096\n"
"v_mov_b32 v22, s32\n"
"v_mov_b32 v23, s33\n"
"v_mul_u32_u24 v20, 64, v1\n"
"s_mov_b64 exec, 0xff\n"
"buffer_store_dwordx2 v[22:23], v20, s[36:39], s60 offen sc1\n"
"s_mov_b64 exec, -1\n"
"L_hnoflag:\n"
"s_waitcnt vmcnt(0)\n"
"s_mov_b64 exec, 1\n"
"ds_add_u32 v25, v24\n"
"s_mov_b64 exec, -1\n"
"s_cmp_lg_u32 s6, 0\n"
"s_cbranch_scc1 L_noep\n"
"s_cmp_lg_u32 s34, 1\n"
"s_cbranch_scc1 L_noep\n"
"v_lshlrev_b32 v20, 4, v1\n"
"s_mov_b32 s60, 36864\n"
"s_mov_b32 s69, 0\n"
"L_gp32:\n"
"buffer_load_dwordx4 v[32:35], v20, s[36:39], s60 offen offset:0 sc1\n"
"buffer_load_dwordx4 v[36:39], v20, s[36:39], s60 offen offset:1024 sc1\n"
"s_waitcnt vmcnt(0)\n"
"v_cmp_ne_u32 vcc, s32, v32\n"
"v_cmp_ne_u32 s[66:67], s33, v33\n"
"s_or_b64 vcc, vcc, s[66:67]\n"
"v_cmp_ne_u32 s[66:67], s32, v34\n"
"s_or_b64 vcc, vcc, s[66:67]\n"
"v_cmp_ne_u32 s[66:67], s33, v35\n"
"s_or_b64 vcc, vcc, s[66:67]\n"
"v_cmp_ne_u32 s[66:67], s32, v36\n"
"s_or_b64 vcc, vcc, s[66:67]\n"
"v_cmp_ne_u32 s[66:67], s33, v37\n"
"s_or_b64 vcc, vcc, s[66:67]\n"
"v_cmp_ne_u32 s[66:67], s32, v38\n"
"s_or_b64 vcc, vcc, s[66:67]\n"
"v_cmp_ne_u32 s[66:67], s33, v39\n"
"s_or_b64 vcc, vcc, s[66:67]\n"
"s_cbranch_vccz L_gpd32\n"
"s_sleep 2\n"
"s_add_u32 s69, s69, 1\n"
"s_cmp_lt_u32 s69, 100000\n"
"s_cbranch_scc1 L_gp32\n"
"L_gpd32:\n"
"s_mov_b32 s60, 0\n"
"v_mov_b32 v22, s32\n"
"v_mov_b32 v23, s33\n"
"v_mul_u32_u24 v20, 0, v1\n"
"s_mov_b64 exec, 0x1\n"
"buffer_store_dwordx2 v[22:23], v20, s[36:39], s60 offen sc1\n"
"s_mov_b64 exec, -1\n"
"L_noep:\n"
"v_mov_b32 v20, 161840\n"
"s_mov_b32 s69, 0\n"
"L_sp33:\n"
"ds_read_b32 v21, v20\n"
"s_waitcnt lgkmcnt(0)\n"
"v_cmp_gt_u32 vcc, 4, v21\n"
"s_cbranch_vccz L_spd33\n"
"s_sleep 1\n"
"s_add_u32 s69, s69, 1\n"
"s_cmp_lt_u32 s69, 100000\n"
"s_cbranch_scc1 L_sp33\n"
"L_spd33:\n"
"v_mov_b32 v20, 161848\n"
"s_mov_b32 s69, 0\n"
"L_sp34:\n"
"ds_read_b32 v21, v20\n"
"s_waitcnt lgkmcnt(0)\n"
"v_cmp_gt_u32 vcc, 4, v21\n"
"s_cbranch_vccz L_spd34\n"
"s_sleep 1\n"
"s_add_u32 s69, s69, 1\n"
"s_cmp_lt_u32 s69, 100000\n"
"s_cbranch_scc1 L_sp34\n"
"L_spd34:\n"
"s_mul_i32 s35, s34, 29952\n"
"s_add_u32 s35, s35, 41984\n"
"s_mov_b32 s48, s22\n"
"s_and_b32 s49, s23, 0xffff\n"
"s_mov_b32 s50, 1966080\n"
"s_mov_b32 s44, s16\n"
"s_and_b32 s45, s17, 0xffff\n"
"s_mov_b32 s46, 819200\n"
"s_mov_b32 s47, 0x20000\n"
"v_lshl_add_u32 v14, v3, 2, v4\n"
"v_mul_u32_u24 v14, 160, v14\n"
"v_add_u32 v14, v14, v5\n"
"v_add_u32 v14, s35, v14\n"
"v_mul_u32_u24 v13, 40, v3\n"
"v_add_u32 v13, v13, v4\n"
"v_mul_u32_u24 v13, 160, v13\n"
"v_add_u32 v13, v13, v5\n"
"v_add_u32 v13, s35, v13\n"
"v_lshlrev_b32 v15, 4, v1\n"
"v_mfma_f32_32x32x16_bf16 v[64:79], v[60:63], v[60:63], 0\n"
"v_mfma_f32_32x32x16_bf16 v[80:95], v[60:63], v[60:63], 0\n"
"v_mfma_f32_32x32x16_bf16 v[96:111], v[60:63], v[60:63], 0\n"
"v_mfma_f32_32x32x16_bf16 v[112:127], v[60:63], v[60:63], 0\n"
"v_mfma_f32_32x32x16_bf16 v[128:143], v[60:63], v[60:63], 0\n"
"v_mfma_f32_32x32x16_bf16 v[144:159], v[60:63], v[60:63], 0\n"
"s_mul_i32 s70, s34, 491520\n"
"s_mul_i32 s65, s34, 204800\n"
"s_mul_i32 s60, s30, 96\n"
"s_mul_i32 s61, s34, 24\n"
"s_add_u32 s60, s60, s61\n"
"s_lshl_b32 s60, s60, 11\n"
"s_add_u32 s71, s60, 3145728\n"
"s_add_u32 s61, s35, 0\n"
"s_mov_b32 m0, s61\n"
"s_nop 0\n"
"buffer_load_dwordx4 v27, s[48:51], s70 offen lds\n"
"s_add_u32 s61, s35, 1024\n"
"s_mov_b32 m0, s61\n"
"s_nop 0\n"
"buffer_load_dwordx4 v28, s[48:51], s70 offen lds\n"
"s_add_u32 s61, s35, 2048\n"
"s_mov_b32 m0, s61\n"
"s_nop 0\n"
"buffer_load_dwordx4 v29, s[48:51], s70 offen lds\n"
"s_add_u32 s61, s35, 3072\n"
"s_mov_b32 m0, s61\n"
"s_nop 0\n"
"buffer_load_dwordx4 v30, s[48:51], s70 offen lds\n"
"s_add_u32 s61, s35, 4096\n"
"s_mov_b32 m0, s61\n"
"s_nop 0\n"
"buffer_load_dwordx4 v31, s[48:51], s70 offen lds\n"
"s_add_u32 s70, s70, 40960\n"
"s_add_u32 s61, s35, 5120\n"
"s_mov_b32 m0, s61\n"
"s_nop 0\n"
"buffer_load_dwordx4 v27, s[48:51], s70 offen lds\n"
"s_add_u32 s61, s35, 6144\n"
"s_mov_b32 m0, s61\n"
"s_nop 0\n"
"buffer_load_dwordx4 v28, s[48:51], s70 offen lds\n"
"s_add_u32 s61, s35, 7168\n"
"s_mov_b32 m0, s61\n"
"s_nop 0\n"
"buffer_load_dwordx4 v29, s[48:51], s70 offen lds\n"
"s_add_u32 s61, s35, 8192\n"
"s_mov_b32 m0, s61\n"
"s_nop 0\n"
"buffer_load_dwordx4 v30, s[48:51], s70 offen lds\n"
"s_add_u32 s61, s35, 9216\n"
"s_mov_b32 m0, s61\n"
"s_nop 0\n"
"buffer_load_dwordx4 v31, s[48:51], s70 offen lds\n"
"s_add_u32 s70, s70, 40960\n"
"s_add_u32 s61, s35, 10240\n"
"s_mov_b32 m0, s61\n"
"s_nop 0\n"
"buffer_load_dwordx4 v27, s[48:51], s70 offen lds\n"
"s_add_u32 s61, s35, 11264\n"
"s_mov_b32 m0, s61\n"
"s_nop 0\n"
"buffer_load_dwordx4 v28, s[48:51], s70 offen lds\n"
"s_add_u32 s61, s35, 12288\n"
"s_mov_b32 m0, s61\n"
"s_nop 0\n"
"buffer_load_dwordx4 v29, s[48:51], s70 offen lds\n"
"s_add_u32 s61, s35, 13312\n"
"s_mov_b32 m0, s61\n"
"s_nop 0\n"
"buffer_load_dwordx4 v30, s[48:51], s70 offen lds\n"
"s_add_u32 s61, s35, 14336\n"
"s_mov_b32 m0, s61\n"
"s_nop 0\n"
"buffer_load_dwordx4 v31, s[48:51], s70 offen lds\n"
"s_add_u32 s70, s70, 40960\n"
"s_setprio 3\n"
"s_mov_b32 s69, 0\n"
"L_sp35:\n"
"ds_read_b32 v26, v7\n"
"s_waitcnt lgkmcnt(0)\n"
"v_cmp_gt_u32 vcc, 7, v26\n"
"s_cbranch_vccz L_spd35\n"
"s_sleep 2\n"
"s_add_u32 s69, s69, 1\n"
"s_cmp_lt_u32 s69, 100000\n"
"s_cbranch_scc1 L_sp35\n"
"L_spd35:\n"
"s_setprio 3\n"
"s_add_u32 s61, s63, 20992\n"
"s_mov_b32 m0, s61\n"
"s_add_u32 s60, s62, 0\n"
"buffer_load_dwordx4 v6, s[36:39], s60 offen sc1 lds\n"
"s_add_u32 s61, s63, 22016\n"
"s_mov_b32 m0, s61\n"
"s_add_u32 s60, s62, 1024\n"
"buffer_load_dwordx4 v6, s[36:39], s60 offen sc1 lds\n"
"s_add_u32 s61, s63, 22592\n"
"s_mov_b32 m0, s61\n"
"s_add_u32 s60, s62, 1600\n"
"buffer_load_dwordx4 v6, s[36:39], s60 offen sc1 lds\n"
"s_add_u32 s62, s62, 10496\n"
"s_lshl_b32 s60, s30, 3\n"
"s_add_u32 s60, s60, s31\n"
"s_lshl_b32 s60, s60, 6\n"
"s_lshl_b32 s61, s34, 4\n"
"s_add_u32 s60, s60, s61\n"
"s_add_u32 s60, s60, 4096\n"
"v_mov_b32 v16, 0\n"
"s_mov_b32 s69, 0\n"
"L_gp36:\n"
"buffer_load_dwordx4 v[20:23], v16, s[36:39], s60 offen offset:0 sc1\n"
"s_waitcnt vmcnt(0)\n"
"v_cmp_ne_u32 vcc, s32, v20\n"
"v_cmp_ne_u32 s[66:67], s33, v21\n"
"s_or_b64 vcc, vcc, s[66:67]\n"
"v_cmp_ne_u32 s[66:67], s32, v22\n"
"s_or_b64 vcc, vcc, s[66:67]\n"
"v_cmp_ne_u32 s[66:67], s33, v23\n"
"s_or_b64 vcc, vcc, s[66:67]\n"
"s_cbranch_vccz L_gpd36\n"
"s_sleep 2\n"
"s_add_u32 s69, s69, 1\n"
"s_cmp_lt_u32 s69, 100000\n"
"s_cbranch_scc1 L_gp36\n"
"L_gpd36:\n"
"s_mov_b64 exec, 1\n"
"ds_add_u32 v25, v24\n"
"s_mov_b64 exec, -1\n"
"buffer_load_dwordx4 v[184:187], v15, s[36:39], s71 offen offset:0 sc1\n"
"buffer_load_dwordx4 v[188:191], v15, s[36:39], s71 offen offset:1024 sc1\n"
"buffer_load_dwordx4 v[192:195], v15, s[36:39], s71 offen offset:2048 sc1\n"
"buffer_load_dwordx4 v[196:199], v15, s[36:39], s71 offen offset:3072 sc1\n"
"s_add_u32 s71, s71, 4096\n"
"buffer_load_dwordx4 v[200:203], v15, s[36:39], s71 offen offset:0 sc1\n"
"buffer_load_dwordx4 v[204:207], v15, s[36:39], s71 offen offset:1024 sc1\n"
"buffer_load_dwordx4 v[208:211], v15, s[36:39], s71 offen offset:2048 sc1\n"
"buffer_load_dwordx4 v[212:215], v15, s[36:39], s71 offen offset:3072 sc1\n"
"s_add_u32 s71, s71, 4096\n"
"buffer_load_dwordx4 v[216:219], v15, s[36:39], s71 offen offset:0 sc1\n"
"buffer_load_dwordx4 v[220:223], v15, s[36:39], s71 offen offset:1024 sc1\n"
"buffer_load_dwordx4 v[224:227], v15, s[36:39], s71 offen offset:2048 sc1\n"
"buffer_load_dwordx4 v[228:231], v15, s[36:39], s71 offen offset:3072 sc1\n"
"s_add_u32 s71, s71, 4096\n"
"s_waitcnt vmcnt(8)\n"
"ds_read_b64_tr_b16 v[160:161], v14 offset:0\n"
"ds_read_b64_tr_b16 v[162:163], v14 offset:1280\n"
"ds_read_b64_tr_b16 v[164:165], v14 offset:64\n"
"ds_read_b64_tr_b16 v[166:167], v14 offset:1344\n"
"ds_read_b64_tr_b16 v[168:169], v14 offset:128\n"
"ds_read_b64_tr_b16 v[170:171], v14 offset:1408\n"
"s_setprio 3\n"
"s_mov_b32 s69, 0\n"
"L_sp37:\n"
"ds_read_b32 v26, v7\n"
"s_waitcnt lgkmcnt(0)\n"
"v_cmp_gt_u32 vcc, 8, v26\n"
"s_cbranch_vccz L_spd37\n"
"s_sleep 2\n"
"s_add_u32 s69, s69, 1\n"
"s_cmp_lt_u32 s69, 100000\n"
"s_cbranch_scc1 L_sp37\n"
"L_spd37:\n"
"s_setprio 3\n"
"s_add_u32 s61, s63, 31488\n"
"s_mov_b32 m0, s61\n"
"s_add_u32 s60, s62, 0\n"
"buffer_load_dwordx4 v6, s[36:39], s60 offen sc1 lds\n"
"s_add_u32 s61, s63, 32512\n"
"s_mov_b32 m0, s61\n"
"s_add_u32 s60, s62, 1024\n"
"buffer_load_dwordx4 v6, s[36:39], s60 offen sc1 lds\n"
"s_add_u32 s61, s63, 33088\n"
"s_mov_b32 m0, s61\n"
"s_add_u32 s60, s62, 1600\n"
"buffer_load_dwordx4 v6, s[36:39], s60 offen sc1 lds\n"
"s_add_u32 s62, s62, 10496\n"
"s_setprio 0\n"
"s_add_u32 s61, s35, 15360\n"
"s_mov_b32 m0, s61\n"
"s_nop 0\n"
"buffer_load_dwordx4 v27, s[48:51], s70 offen lds\n"
"s_add_u32 s61, s35, 16384\n"
"s_mov_b32 m0, s61\n"
"s_nop 0\n"
"buffer_load_dwordx4 v28, s[48:51], s70 offen lds\n"
"s_add_u32 s61, s35, 17408\n"
"s_mov_b32 m0, s61\n"
"s_nop 0\n"
"buffer_load_dwordx4 v29, s[48:51], s70 offen lds\n"
"s_add_u32 s61, s35, 18432\n"
"s_mov_b32 m0, s61\n"
"s_nop 0\n"
"buffer_load_dwordx4 v30, s[48:51], s70 offen lds\n"
"s_add_u32 s61, s35, 19456\n"
"s_mov_b32 m0, s61\n"
"s_nop 0\n"
"buffer_load_dwordx4 v31, s[48:51], s70 offen lds\n"
"s_add_u32 s70, s70, 40960\n"
"buffer_load_dwordx4 v[232:235], v15, s[36:39], s71 offen offset:0 sc1\n"
"buffer_load_dwordx4 v[236:239], v15, s[36:39], s71 offen offset:1024 sc1\n"
"buffer_load_dwordx4 v[240:243], v15, s[36:39], s71 offen offset:2048 sc1\n"
"buffer_load_dwordx4 v[244:247], v15, s[36:39], s71 offen offset:3072 sc1\n"
"s_add_u32 s71, s71, 4096\n"
"ds_read_b64_tr_b16 v[172:173], v14 offset:2560\n"
"ds_read_b64_tr_b16 v[174:175], v14 offset:3840\n"
"ds_read_b64_tr_b16 v[176:177], v14 offset:2624\n"
"ds_read_b64_tr_b16 v[178:179], v14 offset:3904\n"
"ds_read_b64_tr_b16 v[180:181], v14 offset:2688\n"
"ds_read_b64_tr_b16 v[182:183], v14 offset:3968\n"
"v_mfma_f32_32x32x16_bf16 v[64:79], v[160:163], v[184:187], v[64:79]\n"
"v_mfma_f32_32x32x16_bf16 v[80:95], v[160:163], v[188:191], v[80:95]\n"
"v_mfma_f32_32x32x16_bf16 v[96:111], v[164:167], v[184:187], v[96:111]\n"
"v_mfma_f32_32x32x16_bf16 v[112:127], v[164:167], v[188:191], v[112:127]\n"
"v_mfma_f32_32x32x16_bf16 v[128:143], v[168:171], v[184:187], v[128:143]\n"
"v_mfma_f32_32x32x16_bf16 v[144:159], v[168:171], v[188:191], v[144:159]\n"
"s_waitcnt vmcnt(16)\n"
"ds_read_b64_tr_b16 v[160:161], v14 offset:5120\n"
"ds_read_b64_tr_b16 v[162:163], v14 offset:6400\n"
"ds_read_b64_tr_b16 v[164:165], v14 offset:5184\n"
"ds_read_b64_tr_b16 v[166:167], v14 offset:6464\n"
"ds_read_b64_tr_b16 v[168:169], v14 offset:5248\n"
"ds_read_b64_tr_b16 v[170:171], v14 offset:6528\n"
"s_waitcnt lgkmcnt(6)\n"
"v_mfma_f32_32x32x16_bf16 v[64:79], v[172:175], v[192:195], v[64:79]\n"
"v_mfma_f32_32x32x16_bf16 v[80:95], v[172:175], v[196:199], v[80:95]\n"
"v_mfma_f32_32x32x16_bf16 v[96:111], v[176:179], v[192:195], v[96:111]\n"
"v_mfma_f32_32x32x16_bf16 v[112:127], v[176:179], v[196:199], v[112:127]\n"
"v_mfma_f32_32x32x16_bf16 v[128:143], v[180:183], v[192:195], v[128:143]\n"
"v_mfma_f32_32x32x16_bf16 v[144:159], v[180:183], v[196:199], v[144:159]\n"
"s_waitcnt vmcnt(9)\n"
"s_mov_b64 exec, 1\n"
"ds_add_u32 v25, v24\n"
"s_mov_b64 exec, -1\n"
"s_add_u32 s61, s35, 20480\n"
"s_mov_b32 m0, s61\n"
"s_nop 0\n"
"buffer_load_dwordx4 v27, s[48:51], s70 offen lds\n"
"s_add_u32 s61, s35, 21504\n"
"s_mov_b32 m0, s61\n"
"s_nop 0\n"
"buffer_load_dwordx4 v28, s[48:51], s70 offen lds\n"
"s_add_u32 s61, s35, 22528\n"
"s_mov_b32 m0, s61\n"
"s_nop 0\n"
"buffer_load_dwordx4 v29, s[48:51], s70 offen lds\n"
"s_add_u32 s61, s35, 23552\n"
"s_mov_b32 m0, s61\n"
"s_nop 0\n"
"buffer_load_dwordx4 v30, s[48:51], s70 offen lds\n"
"s_add_u32 s61, s35, 24576\n"
"s_mov_b32 m0, s61\n"
"s_nop 0\n"
"buffer_load_dwordx4 v31, s[48:51], s70 offen lds\n"
"s_add_u32 s70, s70, 40960\n"
"buffer_load_dwordx4 v[184:187], v15, s[36:39], s71 offen offset:0 sc1\n"
"buffer_load_dwordx4 v[188:191], v15, s[36:39], s71 offen offset:1024 sc1\n"
"buffer_load_dwordx4 v[192:195], v15, s[36:39], s71 offen offset:2048 sc1\n"
"buffer_load_dwordx4 v[196:199], v15, s[36:39], s71 offen offset:3072 sc1\n"
"s_add_u32 s71, s71, 4096\n"
"ds_read_b64_tr_b16 v[172:173], v14 offset:7680\n"
"ds_read_b64_tr_b16 v[174:175], v14 offset:8960\n"
"ds_read_b64_tr_b16 v[176:177], v14 offset:7744\n"
"ds_read_b64_tr_b16 v[178:179], v14 offset:9024\n"
"ds_read_b64_tr_b16 v[180:181], v14 offset:7808\n"
"ds_read_b64_tr_b16 v[182:183], v14 offset:9088\n"
"s_waitcnt lgkmcnt(6)\n"
"v_mfma_f32_32x32x16_bf16 v[64:79], v[160:163], v[200:203], v[64:79]\n"
"v_mfma_f32_32x32x16_bf16 v[80:95], v[160:163], v[204:207], v[80:95]\n"
"v_mfma_f32_32x32x16_bf16 v[96:111], v[164:167], v[200:203], v[96:111]\n"
"v_mfma_f32_32x32x16_bf16 v[112:127], v[164:167], v[204:207], v[112:127]\n"
"v_mfma_f32_32x32x16_bf16 v[128:143], v[168:171], v[200:203], v[128:143]\n"
"v_mfma_f32_32x32x16_bf16 v[144:159], v[168:171], v[204:207], v[144:159]\n"
"ds_read_b64_tr_b16 v[160:161], v14 offset:10240\n"
"ds_read_b64_tr_b16 v[162:163], v14 offset:11520\n"
"ds_read_b64_tr_b16 v[164:165], v14 offset:10304\n"
"ds_read_b64_tr_b16 v[166:167], v14 offset:11584\n"
"ds_read_b64_tr_b16 v[168:169], v14 offset:10368\n"
"ds_read_b64_tr_b16 v[170:171], v14 offset:11648\n"
"s_waitcnt lgkmcnt(6)\n"
"v_mfma_f32_32x32x16_bf16 v[64:79], v[172:175], v[208:211], v[64:79]\n"
"v_mfma_f32_32x32x16_bf16 v[80:95], v[172:175], v[212:215], v[80:95]\n"
"v_mfma_f32_32x32x16_bf16 v[96:111], v[176:179], v[208:211], v[96:111]\n"
"v_mfma_f32_32x32x16_bf16 v[112:127], v[176:179], v[212:215], v[112:127]\n"
"v_mfma_f32_32x32x16_bf16 v[128:143], v[180:183], v[208:211], v[128:143]\n"
"v_mfma_f32_32x32x16_bf16 v[144:159], v[180:183], v[212:215], v[144:159]\n"
"s_setprio 3\n"
"s_mov_b32 s69, 0\n"
"L_sp38:\n"
"ds_read_b32 v26, v7\n"
"s_waitcnt lgkmcnt(0)\n"
"v_cmp_gt_u32 vcc, 9, v26\n"
"s_cbranch_vccz L_spd38\n"
"s_sleep 2\n"
"s_add_u32 s69, s69, 1\n"
"s_cmp_lt_u32 s69, 100000\n"
"s_cbranch_scc1 L_sp38\n"
"L_spd38:\n"
"s_setprio 3\n"
"s_add_u32 s61, s63, 0\n"
"s_mov_b32 m0, s61\n"
"s_add_u32 s60, s62, 0\n"
"buffer_load_dwordx4 v6, s[36:39], s60 offen sc1 lds\n"
"s_add_u32 s61, s63, 1024\n"
"s_mov_b32 m0, s61\n"
"s_add_u32 s60, s62, 1024\n"
"buffer_load_dwordx4 v6, s[36:39], s60 offen sc1 lds\n"
"s_add_u32 s61, s63, 1600\n"
"s_mov_b32 m0, s61\n"
"s_add_u32 s60, s62, 1600\n"
"buffer_load_dwordx4 v6, s[36:39], s60 offen sc1 lds\n"
"s_add_u32 s62, s62, 10496\n"
"s_setprio 0\n"
"s_add_u32 s61, s35, 0\n"
"s_mov_b32 m0, s61\n"
"s_nop 0\n"
"buffer_load_dwordx4 v27, s[48:51], s70 offen lds\n"
"s_add_u32 s61, s35, 1024\n"
"s_mov_b32 m0, s61\n"
"s_nop 0\n"
"buffer_load_dwordx4 v28, s[48:51], s70 offen lds\n"
"s_add_u32 s61, s35, 2048\n"
"s_mov_b32 m0, s61\n"
"s_nop 0\n"
"buffer_load_dwordx4 v29, s[48:51], s70 offen lds\n"
"s_add_u32 s61, s35, 3072\n"
"s_mov_b32 m0, s61\n"
"s_nop 0\n"
"buffer_load_dwordx4 v30, s[48:51], s70 offen lds\n"
"s_add_u32 s61, s35, 4096\n"
"s_mov_b32 m0, s61\n"
"s_nop 0\n"
"buffer_load_dwordx4 v31, s[48:51], s70 offen lds\n"
"s_add_u32 s70, s70, 40960\n"
"buffer_load_dwordx4 v[200:203], v15, s[36:39], s71 offen offset:0 sc1\n"
"buffer_load_dwordx4 v[204:207], v15, s[36:39], s71 offen offset:1024 sc1\n"
"buffer_load_dwordx4 v[208:211], v15, s[36:39], s71 offen offset:2048 sc1\n"
"buffer_load_dwordx4 v[212:215], v15, s[36:39], s71 offen offset:3072 sc1\n"
"s_add_u32 s71, s71, 4096\n"
"ds_read_b64_tr_b16 v[172:173], v14 offset:12800\n"
"ds_read_b64_tr_b16 v[174:175], v14 offset:14080\n"
"ds_read_b64_tr_b16 v[176:177], v14 offset:12864\n"
"ds_read_b64_tr_b16 v[178:179], v14 offset:14144\n"
"ds_read_b64_tr_b16 v[180:181], v14 offset:12928\n"
"ds_read_b64_tr_b16 v[182:183], v14 offset:14208\n"
"v_mfma_f32_32x32x16_bf16 v[64:79], v[160:163], v[216:219], v[64:79]\n"
"v_mfma_f32_32x32x16_bf16 v[80:95], v[160:163], v[220:223], v[80:95]\n"
"v_mfma_f32_32x32x16_bf16 v[96:111], v[164:167], v[216:219], v[96:111]\n"
"v_mfma_f32_32x32x16_bf16 v[112:127], v[164:167], v[220:223], v[112:127]\n"
"v_mfma_f32_32x32x16_bf16 v[128:143], v[168:171], v[216:219], v[128:143]\n"
"v_mfma_f32_32x32x16_bf16 v[144:159], v[168:171], v[220:223], v[144:159]\n"
"s_waitcnt vmcnt(21)\n"
"ds_read_b64_tr_b16 v[160:161], v14 offset:15360\n"
"ds_read_b64_tr_b16 v[162:163], v14 offset:16640\n"
"ds_read_b64_tr_b16 v[164:165], v14 offset:15424\n"
"ds_read_b64_tr_b16 v[166:167], v14 offset:16704\n"
"ds_read_b64_tr_b16 v[168:169], v14 offset:15488\n"
"ds_read_b64_tr_b16 v[170:171], v14 offset:16768\n"
"s_waitcnt lgkmcnt(6)\n"
"v_mfma_f32_32x32x16_bf16 v[64:79], v[172:175], v[224:227], v[64:79]\n"
"v_mfma_f32_32x32x16_bf16 v[80:95], v[172:175], v[228:231], v[80:95]\n"
"v_mfma_f32_32x32x16_bf16 v[96:111], v[176:179], v[224:227], v[96:111]\n"
"v_mfma_f32_32x32x16_bf16 v[112:127], v[176:179], v[228:231], v[112:127]\n"
"v_mfma_f32_32x32x16_bf16 v[128:143], v[180:183], v[224:227], v[128:143]\n"
"v_mfma_f32_32x32x16_bf16 v[144:159], v[180:183], v[228:231], v[144:159]\n"
"s_waitcnt vmcnt(9)\n"
"s_mov_b64 exec, 1\n"
"ds_add_u32 v25, v24\n"
"s_mov_b64 exec, -1\n"
"s_add_u32 s61, s35, 5120\n"
"s_mov_b32 m0, s61\n"
"s_nop 0\n"
"buffer_load_dwordx4 v27, s[48:51], s70 offen lds\n"
"s_add_u32 s61, s35, 6144\n"
"s_mov_b32 m0, s61\n"
"s_nop 0\n"
"buffer_load_dwordx4 v28, s[48:51], s70 offen lds\n"
"s_add_u32 s61, s35, 7168\n"
"s_mov_b32 m0, s61\n"
"s_nop 0\n"
"buffer_load_dwordx4 v29, s[48:51], s70 offen lds\n"
"s_add_u32 s61, s35, 8192\n"
"s_mov_b32 m0, s61\n"
"s_nop 0\n"
"buffer_load_dwordx4 v30, s[48:51], s70 offen lds\n"
"s_add_u32 s61, s35, 9216\n"
"s_mov_b32 m0, s61\n"
"s_nop 0\n"
"buffer_load_dwordx4 v31, s[48:51], s70 offen lds\n"
"s_add_u32 s70, s70, 40960\n"
"buffer_load_dwordx4 v[216:219], v15, s[36:39], s71 offen offset:0 sc1\n"
"buffer_load_dwordx4 v[220:223], v15, s[36:39], s71 offen offset:1024 sc1\n"
"buffer_load_dwordx4 v[224:227], v15, s[36:39], s71 offen offset:2048 sc1\n"
"buffer_load_dwordx4 v[228:231], v15, s[36:39], s71 offen offset:3072 sc1\n"
"s_add_u32 s71, s71, 4096\n"
"ds_read_b64_tr_b16 v[172:173], v14 offset:17920\n"
"ds_read_b64_tr_b16 v[174:175], v14 offset:19200\n"
"ds_read_b64_tr_b16 v[176:177], v14 offset:17984\n"
"ds_read_b64_tr_b16 v[178:179], v14 offset:19264\n"
"ds_read_b64_tr_b16 v[180:181], v14 offset:18048\n"
"ds_read_b64_tr_b16 v[182:183], v14 offset:19328\n"
"s_waitcnt lgkmcnt(6)\n"
"v_mfma_f32_32x32x16_bf16 v[64:79], v[160:163], v[232:235], v[64:79]\n"
"v_mfma_f32_32x32x16_bf16 v[80:95], v[160:163], v[236:239], v[80:95]\n"
"v_mfma_f32_32x32x16_bf16 v[96:111], v[164:167], v[232:235], v[96:111]\n"
"v_mfma_f32_32x32x16_bf16 v[112:127], v[164:167], v[236:239], v[112:127]\n"
"v_mfma_f32_32x32x16_bf16 v[128:143], v[168:171], v[232:235], v[128:143]\n"
"v_mfma_f32_32x32x16_bf16 v[144:159], v[168:171], v[236:239], v[144:159]\n"
"ds_read_b64_tr_b16 v[160:161], v14 offset:20480\n"
"ds_read_b64_tr_b16 v[162:163], v14 offset:21760\n"
"ds_read_b64_tr_b16 v[164:165], v14 offset:20544\n"
"ds_read_b64_tr_b16 v[166:167], v14 offset:21824\n"
"ds_read_b64_tr_b16 v[168:169], v14 offset:20608\n"
"ds_read_b64_tr_b16 v[170:171], v14 offset:21888\n"
"s_waitcnt lgkmcnt(6)\n"
"v_mfma_f32_32x32x16_bf16 v[64:79], v[172:175], v[240:243], v[64:79]\n"
"v_mfma_f32_32x32x16_bf16 v[80:95], v[172:175], v[244:247], v[80:95]\n"
"v_mfma_f32_32x32x16_bf16 v[96:111], v[176:179], v[240:243], v[96:111]\n"
"v_mfma_f32_32x32x16_bf16 v[112:127], v[176:179], v[244:247], v[112:127]\n"
"v_mfma_f32_32x32x16_bf16 v[128:143], v[180:183], v[240:243], v[128:143]\n"
"v_mfma_f32_32x32x16_bf16 v[144:159], v[180:183], v[244:247], v[144:159]\n"
"s_setprio 3\n"
"s_mov_b32 s69, 0\n"
"L_sp39:\n"
"ds_read_b32 v26, v7\n"
"s_waitcnt lgkmcnt(0)\n"
"v_cmp_gt_u32 vcc, 10, v26\n"
"s_cbranch_vccz L_spd39\n"
"s_sleep 2\n"
"s_add_u32 s69, s69, 1\n"
"s_cmp_lt_u32 s69, 100000\n"
"s_cbranch_scc1 L_sp39\n"
"L_spd39:\n"
"s_setprio 3\n"
"s_add_u32 s61, s63, 10496\n"
"s_mov_b32 m0, s61\n"
"s_add_u32 s60, s62, 0\n"
"buffer_load_dwordx4 v6, s[36:39], s60 offen sc1 lds\n"
"s_add_u32 s61, s63, 11520\n"
"s_mov_b32 m0, s61\n"
"s_add_u32 s60, s62, 1024\n"
"buffer_load_dwordx4 v6, s[36:39], s60 offen sc1 lds\n"
"s_add_u32 s61, s63, 12096\n"
"s_mov_b32 m0, s61\n"
"s_add_u32 s60, s62, 1600\n"
"buffer_load_dwordx4 v6, s[36:39], s60 offen sc1 lds\n"
"s_add_u32 s62, s62, 10496\n"
"s_setprio 0\n"
"s_add_u32 s61, s35, 10240\n"
"s_mov_b32 m0, s61\n"
"s_nop 0\n"
"buffer_load_dwordx4 v27, s[48:51], s70 offen lds\n"
"s_add_u32 s61, s35, 11264\n"
"s_mov_b32 m0, s61\n"
"s_nop 0\n"
"buffer_load_dwordx4 v28, s[48:51], s70 offen lds\n"
"s_add_u32 s61, s35, 12288\n"
"s_mov_b32 m0, s61\n"
"s_nop 0\n"
"buffer_load_dwordx4 v29, s[48:51], s70 offen lds\n"
"s_add_u32 s61, s35, 13312\n"
"s_mov_b32 m0, s61\n"
"s_nop 0\n"
"buffer_load_dwordx4 v30, s[48:51], s70 offen lds\n"
"s_add_u32 s61, s35, 14336\n"
"s_mov_b32 m0, s61\n"
"s_nop 0\n"
"buffer_load_dwordx4 v31, s[48:51], s70 offen lds\n"
"s_add_u32 s70, s70, 40960\n"
"buffer_load_dwordx4 v[232:235], v15, s[36:39], s71 offen offset:0 sc1\n"
"buffer_load_dwordx4 v[236:239], v15, s[36:39], s71 offen offset:1024 sc1\n"
"buffer_load_dwordx4 v[240:243], v15, s[36:39], s71 offen offset:2048 sc1\n"
"buffer_load_dwordx4 v[244:247], v15, s[36:39], s71 offen offset:3072 sc1\n"
"s_add_u32 s71, s71, 4096\n"
"ds_read_b64_tr_b16 v[172:173], v14 offset:23040\n"
"ds_read_b64_tr_b16 v[174:175], v14 offset:24320\n"
"ds_read_b64_tr_b16 v[176:177], v14 offset:23104\n"
"ds_read_b64_tr_b16 v[178:179], v14 offset:24384\n"
"ds_read_b64_tr_b16 v[180:181], v14 offset:23168\n"
"ds_read_b64_tr_b16 v[182:183], v14 offset:24448\n"
"v_mfma_f32_32x32x16_bf16 v[64:79], v[160:163], v[184:187], v[64:79]\n"
"v_mfma_f32_32x32x16_bf16 v[80:95], v[160:163], v[188:191], v[80:95]\n"
"v_mfma_f32_32x32x16_bf16 v[96:111], v[164:167], v[184:187], v[96:111]\n"
"v_mfma_f32_32x32x16_bf16 v[112:127], v[164:167], v[188:191], v[112:127]\n"
"v_mfma_f32_32x32x16_bf16 v[128:143], v[168:171], v[184:187], v[128:143]\n"
"v_mfma_f32_32x32x16_bf16 v[144:159], v[168:171], v[188:191], v[144:159]\n"
"s_waitcnt vmcnt(21)\n"
"ds_read_b64_tr_b16 v[160:161], v14 offset:0\n"
"ds_read_b64_tr_b16 v[162:163], v14 offset:1280\n"
"ds_read_b64_tr_b16 v[164:165], v14 offset:64\n"
"ds_read_b64_tr_b16 v[166:167], v14 offset:1344\n"
"ds_read_b64_tr_b16 v[168:169], v14 offset:128\n"
"ds_read_b64_tr_b16 v[170:171], v14 offset:1408\n"
"s_waitcnt lgkmcnt(6)\n"
"v_mfma_f32_32x32x16_bf16 v[64:79], v[172:175], v[192:195], v[64:79]\n"
"v_mfma_f32_32x32x16_bf16 v[80:95], v[172:175], v[196:199], v[80:95]\n"
"v_mfma_f32_32x32x16_bf16 v[96:111], v[176:179], v[192:195], v[96:111]\n"
"v_mfma_f32_32x32x16_bf16 v[112:127], v[176:179], v[196:199], v[112:127]\n"
"v_mfma_f32_32x32x16_bf16 v[128:143], v[180:183], v[192:195], v[128:143]\n"
"v_mfma_f32_32x32x16_bf16 v[144:159], v[180:183], v[196:199], v[144:159]\n"
"s_waitcnt vmcnt(9)\n"
"s_mov_b64 exec, 1\n"
"ds_add_u32 v25, v24\n"
"s_mov_b64 exec, -1\n"
"s_add_u32 s61, s35, 15360\n"
"s_mov_b32 m0, s61\n"
"s_nop 0\n"
"buffer_load_dwordx4 v27, s[48:51], s70 offen lds\n"
"s_add_u32 s61, s35, 16384\n"
"s_mov_b32 m0, s61\n"
"s_nop 0\n"
"buffer_load_dwordx4 v28, s[48:51], s70 offen lds\n"
"s_add_u32 s61, s35, 17408\n"
"s_mov_b32 m0, s61\n"
"s_nop 0\n"
"buffer_load_dwordx4 v29, s[48:51], s70 offen lds\n"
"s_add_u32 s61, s35, 18432\n"
"s_mov_b32 m0, s61\n"
"s_nop 0\n"
"buffer_load_dwordx4 v30, s[48:51], s70 offen lds\n"
"s_add_u32 s61, s35, 19456\n"
"s_mov_b32 m0, s61\n"
"s_nop 0\n"
"buffer_load_dwordx4 v31, s[48:51], s70 offen lds\n"
"s_add_u32 s70, s70, 40960\n"
"buffer_load_dwordx4 v[184:187], v15, s[36:39], s71 offen offset:0 sc1\n"
"buffer_load_dwordx4 v[188:191], v15, s[36:39], s71 offen offset:1024 sc1\n"
"buffer_load_dwordx4 v[192:195], v15, s[36:39], s71 offen offset:2048 sc1\n"
"buffer_load_dwordx4 v[196:199], v15, s[36:39], s71 offen offset:3072 sc1\n"
"s_add_u32 s71, s71, 4096\n"
"ds_read_b64_tr_b16 v[172:173], v14 offset:2560\n"
"ds_read_b64_tr_b16 v[174:175], v14 offset:3840\n"
"ds_read_b64_tr_b16 v[176:177], v14 offset:2624\n"
"ds_read_b64_tr_b16 v[178:179], v14 offset:3904\n"
"ds_read_b64_tr_b16 v[180:181], v14 offset:2688\n"
"ds_read_b64_tr_b16 v[182:183], v14 offset:3968\n"
"s_waitcnt lgkmcnt(6)\n"
"v_mfma_f32_32x32x16_bf16 v[64:79], v[160:163], v[200:203], v[64:79]\n"
"v_mfma_f32_32x32x16_bf16 v[80:95], v[160:163], v[204:207], v[80:95]\n"
"v_mfma_f32_32x32x16_bf16 v[96:111], v[164:167], v[200:203], v[96:111]\n"
"v_mfma_f32_32x32x16_bf16 v[112:127], v[164:167], v[204:207], v[112:127]\n"
"v_mfma_f32_32x32x16_bf16 v[128:143], v[168:171], v[200:203], v[128:143]\n"
"v_mfma_f32_32x32x16_bf16 v[144:159], v[168:171], v[204:207], v[144:159]\n"
"ds_read_b64_tr_b16 v[160:161], v14 offset:5120\n"
"ds_read_b64_tr_b16 v[162:163], v14 offset:6400\n"
"ds_read_b64_tr_b16 v[164:165], v14 offset:5184\n"
"ds_read_b64_tr_b16 v[166:167], v14 offset:6464\n"
"ds_read_b64_tr_b16 v[168:169], v14 offset:5248\n"
"ds_read_b64_tr_b16 v[170:171], v14 offset:6528\n"
"s_waitcnt lgkmcnt(6)\n"
"v_mfma_f32_32x32x16_bf16 v[64:79], v[172:175], v[208:211], v[64:79]\n"
"v_mfma_f32_32x32x16_bf16 v[80:95], v[172:175], v[212:215], v[80:95]\n"
"v_mfma_f32_32x32x16_bf16 v[96:111], v[176:179], v[208:211], v[96:111]\n"
"v_mfma_f32_32x32x16_bf16 v[112:127], v[176:179], v[212:215], v[112:127]\n"
"v_mfma_f32_32x32x16_bf16 v[128:143], v[180:183], v[208:211], v[128:143]\n"
"v_mfma_f32_32x32x16_bf16 v[144:159], v[180:183], v[212:215], v[144:159]\n"
"s_setprio 3\n"
"s_mov_b32 s69, 0\n"
"L_sp40:\n"
"ds_read_b32 v26, v7\n"
"s_waitcnt lgkmcnt(0)\n"
"v_cmp_gt_u32 vcc, 11, v26\n"
"s_cbranch_vccz L_spd40\n"
"s_sleep 2\n"
"s_add_u32 s69, s69, 1\n"
"s_cmp_lt_u32 s69, 100000\n"
"s_cbranch_scc1 L_sp40\n"
"L_spd40:\n"
"s_setprio 3\n"
"s_add_u32 s61, s63, 20992\n"
"s_mov_b32 m0, s61\n"
"s_add_u32 s60, s62, 0\n"
"buffer_load_dwordx4 v6, s[36:39], s60 offen sc1 lds\n"
"s_add_u32 s61, s63, 22016\n"
"s_mov_b32 m0, s61\n"
"s_add_u32 s60, s62, 1024\n"
"buffer_load_dwordx4 v6, s[36:39], s60 offen sc1 lds\n"
"s_add_u32 s61, s63, 22592\n"
"s_mov_b32 m0, s61\n"
"s_add_u32 s60, s62, 1600\n"
"buffer_load_dwordx4 v6, s[36:39], s60 offen sc1 lds\n"
"s_add_u32 s62, s62, 10496\n"
"s_setprio 0\n"
"s_add_u32 s61, s35, 20480\n"
"s_mov_b32 m0, s61\n"
"s_nop 0\n"
"buffer_load_dwordx4 v27, s[48:51], s70 offen lds\n"
"s_add_u32 s61, s35, 21504\n"
"s_mov_b32 m0, s61\n"
"s_nop 0\n"
"buffer_load_dwordx4 v28, s[48:51], s70 offen lds\n"
"s_add_u32 s61, s35, 22528\n"
"s_mov_b32 m0, s61\n"
"s_nop 0\n"
"buffer_load_dwordx4 v29, s[48:51], s70 offen lds\n"
"s_add_u32 s61, s35, 23552\n"
"s_mov_b32 m0, s61\n"
"s_nop 0\n"
"buffer_load_dwordx4 v30, s[48:51], s70 offen lds\n"
"s_add_u32 s61, s35, 24576\n"
"s_mov_b32 m0, s61\n"
"s_nop 0\n"
"buffer_load_dwordx4 v31, s[48:51], s70 offen lds\n"
"s_add_u32 s70, s70, 40960\n"
"buffer_load_dwordx4 v[200:203], v15, s[36:39], s71 offen offset:0 sc1\n"
"buffer_load_dwordx4 v[204:207], v15, s[36:39], s71 offen offset:1024 sc1\n"
"buffer_load_dwordx4 v[208:211], v15, s[36:39], s71 offen offset:2048 sc1\n"
"buffer_load_dwordx4 v[212:215], v15, s[36:39], s71 offen offset:3072 sc1\n"
"s_add_u32 s71, s71, 4096\n"
"ds_read_b64_tr_b16 v[172:173], v14 offset:7680\n"
"ds_read_b64_tr_b16 v[174:175], v14 offset:8960\n"
"ds_read_b64_tr_b16 v[176:177], v14 offset:7744\n"
"ds_read_b64_tr_b16 v[178:179], v14 offset:9024\n"
"ds_read_b64_tr_b16 v[180:181], v14 offset:7808\n"
"ds_read_b64_tr_b16 v[182:183], v14 offset:9088\n"
"v_mfma_f32_32x32x16_bf16 v[64:79], v[160:163], v[216:219], v[64:79]\n"
"v_mfma_f32_32x32x16_bf16 v[80:95], v[160:163], v[220:223], v[80:95]\n"
"v_mfma_f32_32x32x16_bf16 v[96:111], v[164:167], v[216:219], v[96:111]\n"
"v_mfma_f32_32x32x16_bf16 v[112:127], v[164:167], v[220:223], v[112:127]\n"
"v_mfma_f32_32x32x16_bf16 v[128:143], v[168:171], v[216:219], v[128:143]\n"
"v_mfma_f32_32x32x16_bf16 v[144:159], v[168:171], v[220:223], v[144:159]\n"
"s_waitcnt vmcnt(21)\n"
"ds_read_b64_tr_b16 v[160:161], v14 offset:10240\n"
"ds_read_b64_tr_b16 v[162:163], v14 offset:11520\n"
"ds_read_b64_tr_b16 v[164:165], v14 offset:10304\n"
"ds_read_b64_tr_b16 v[166:167], v14 offset:11584\n"
"ds_read_b64_tr_b16 v[168:169], v14 offset:10368\n"
"ds_read_b64_tr_b16 v[170:171], v14 offset:11648\n"
"s_waitcnt lgkmcnt(6)\n"
"v_mfma_f32_32x32x16_bf16 v[64:79], v[172:175], v[224:227], v[64:79]\n"
"v_mfma_f32_32x32x16_bf16 v[80:95], v[172:175], v[228:231], v[80:95]\n"
"v_mfma_f32_32x32x16_bf16 v[96:111], v[176:179], v[224:227], v[96:111]\n"
"v_mfma_f32_32x32x16_bf16 v[112:127], v[176:179], v[228:231], v[112:127]\n"
"v_mfma_f32_32x32x16_bf16 v[128:143], v[180:183], v[224:227], v[128:143]\n"
"v_mfma_f32_32x32x16_bf16 v[144:159], v[180:183], v[228:231], v[144:159]\n"
"s_waitcnt vmcnt(9)\n"
"s_mov_b64 exec, 1\n"
"ds_add_u32 v25, v24\n"
"s_mov_b64 exec, -1\n"
"s_add_u32 s61, s35, 0\n"
"s_mov_b32 m0, s61\n"
"s_nop 0\n"
"buffer_load_dwordx4 v27, s[48:51], s70 offen lds\n"
"s_add_u32 s61, s35, 1024\n"
"s_mov_b32 m0, s61\n"
"s_nop 0\n"
"buffer_load_dwordx4 v28, s[48:51], s70 offen lds\n"
"s_add_u32 s61, s35, 2048\n"
"s_mov_b32 m0, s61\n"
"s_nop 0\n"
"buffer_load_dwordx4 v29, s[48:51], s70 offen lds\n"
"s_add_u32 s61, s35, 3072\n"
"s_mov_b32 m0, s61\n"
"s_nop 0\n"
"buffer_load_dwordx4 v30, s[48:51], s70 offen lds\n"
"s_add_u32 s61, s35, 4096\n"
"s_mov_b32 m0, s61\n"
"s_nop 0\n"
"buffer_load_dwordx4 v31, s[48:51], s70 offen lds\n"
"s_add_u32 s70, s70, 40960\n"
"buffer_load_dwordx4 v[216:219], v15, s[36:39], s71 offen offset:0 sc1\n"
"buffer_load_dwordx4 v[220:223], v15, s[36:39], s71 offen offset:1024 sc1\n"
"buffer_load_dwordx4 v[224:227], v15, s[36:39], s71 offen offset:2048 sc1\n"
"buffer_load_dwordx4 v[228:231], v15, s[36:39], s71 offen offset:3072 sc1\n"
"s_add_u32 s71, s71, 4096\n"
"ds_read_b64_tr_b16 v[172:173], v14 offset:12800\n"
"ds_read_b64_tr_b16 v[174:175], v14 offset:14080\n"
"ds_read_b64_tr_b16 v[176:177], v14 offset:12864\n"
"ds_read_b64_tr_b16 v[178:179], v14 offset:14144\n"
"ds_read_b64_tr_b16 v[180:181], v14 offset:12928\n"
"ds_read_b64_tr_b16 v[182:183], v14 offset:14208\n"
"s_waitcnt lgkmcnt(6)\n"
"v_mfma_f32_32x32x16_bf16 v[64:79], v[160:163], v[232:235], v[64:79]\n"
"v_mfma_f32_32x32x16_bf16 v[80:95], v[160:163], v[236:239], v[80:95]\n"
"v_mfma_f32_32x32x16_bf16 v[96:111], v[164:167], v[232:235], v[96:111]\n"
"v_mfma_f32_32x32x16_bf16 v[112:127], v[164:167], v[236:239], v[112:127]\n"
"v_mfma_f32_32x32x16_bf16 v[128:143], v[168:171], v[232:235], v[128:143]\n"
"v_mfma_f32_32x32x16_bf16 v[144:159], v[168:171], v[236:239], v[144:159]\n"
"ds_read_b64_tr_b16 v[160:161], v14 offset:15360\n"
"ds_read_b64_tr_b16 v[162:163], v14 offset:16640\n"
"ds_read_b64_tr_b16 v[164:165], v14 offset:15424\n"
"ds_read_b64_tr_b16 v[166:167], v14 offset:16704\n"
"ds_read_b64_tr_b16 v[168:169], v14 offset:15488\n"
"ds_read_b64_tr_b16 v[170:171], v14 offset:16768\n"
"s_waitcnt lgkmcnt(6)\n"
"v_mfma_f32_32x32x16_bf16 v[64:79], v[172:175], v[240:243], v[64:79]\n"
"v_mfma_f32_32x32x16_bf16 v[80:95], v[172:175], v[244:247], v[80:95]\n"
"v_mfma_f32_32x32x16_bf16 v[96:111], v[176:179], v[240:243], v[96:111]\n"
"v_mfma_f32_32x32x16_bf16 v[112:127], v[176:179], v[244:247], v[112:127]\n"
"v_mfma_f32_32x32x16_bf16 v[128:143], v[180:183], v[240:243], v[128:143]\n"
"v_mfma_f32_32x32x16_bf16 v[144:159], v[180:183], v[244:247], v[144:159]\n"
"s_setprio 3\n"
"s_mov_b32 s69, 0\n"
"L_sp41:\n"
"ds_read_b32 v26, v7\n"
"s_waitcnt lgkmcnt(0)\n"
"v_cmp_gt_u32 vcc, 12, v26\n"
"s_cbranch_vccz L_spd41\n"
"s_sleep 2\n"
"s_add_u32 s69, s69, 1\n"
"s_cmp_lt_u32 s69, 100000\n"
"s_cbranch_scc1 L_sp41\n"
"L_spd41:\n"
"s_setprio 3\n"
"s_add_u32 s61, s63, 31488\n"
"s_mov_b32 m0, s61\n"
"s_add_u32 s60, s62, 0\n"
"buffer_load_dwordx4 v6, s[36:39], s60 offen sc1 lds\n"
"s_add_u32 s61, s63, 32512\n"
"s_mov_b32 m0, s61\n"
"s_add_u32 s60, s62, 1024\n"
"buffer_load_dwordx4 v6, s[36:39], s60 offen sc1 lds\n"
"s_add_u32 s61, s63, 33088\n"
"s_mov_b32 m0, s61\n"
"s_add_u32 s60, s62, 1600\n"
"buffer_load_dwordx4 v6, s[36:39], s60 offen sc1 lds\n"
"s_add_u32 s62, s62, 10496\n"
"s_setprio 0\n"
"s_add_u32 s61, s35, 5120\n"
"s_mov_b32 m0, s61\n"
"s_nop 0\n"
"buffer_load_dwordx4 v27, s[48:51], s70 offen lds\n"
"s_add_u32 s61, s35, 6144\n"
"s_mov_b32 m0, s61\n"
"s_nop 0\n"
"buffer_load_dwordx4 v28, s[48:51], s70 offen lds\n"
"s_add_u32 s61, s35, 7168\n"
"s_mov_b32 m0, s61\n"
"s_nop 0\n"
"buffer_load_dwordx4 v29, s[48:51], s70 offen lds\n"
"s_add_u32 s61, s35, 8192\n"
"s_mov_b32 m0, s61\n"
"s_nop 0\n"
"buffer_load_dwordx4 v30, s[48:51], s70 offen lds\n"
"s_add_u32 s61, s35, 9216\n"
"s_mov_b32 m0, s61\n"
"s_nop 0\n"
"buffer_load_dwordx4 v31, s[48:51], s70 offen lds\n"
"s_add_u32 s70, s70, 40960\n"
"buffer_load_dwordx4 v[232:235], v15, s[36:39], s71 offen offset:0 sc1\n"
"buffer_load_dwordx4 v[236:239], v15, s[36:39], s71 offen offset:1024 sc1\n"
"buffer_load_dwordx4 v[240:243], v15, s[36:39], s71 offen offset:2048 sc1\n"
"buffer_load_dwordx4 v[244:247], v15, s[36:39], s71 offen offset:3072 sc1\n"
"s_add_u32 s71, s71, 4096\n"
"ds_read_b64_tr_b16 v[172:173], v14 offset:17920\n"
"ds_read_b64_tr_b16 v[174:175], v14 offset:19200\n"
"ds_read_b64_tr_b16 v[176:177], v14 offset:17984\n"
"ds_read_b64_tr_b16 v[178:179], v14 offset:19264\n"
"ds_read_b64_tr_b16 v[180:181], v14 offset:18048\n"
"ds_read_b64_tr_b16 v[182:183], v14 offset:19328\n"
"v_mfma_f32_32x32x16_bf16 v[64:79], v[160:163], v[184:187], v[64:79]\n"
"v_mfma_f32_32x32x16_bf16 v[80:95], v[160:163], v[188:191], v[80:95]\n"
"v_mfma_f32_32x32x16_bf16 v[96:111], v[164:167], v[184:187], v[96:111]\n"
"v_mfma_f32_32x32x16_bf16 v[112:127], v[164:167], v[188:191], v[112:127]\n"
"v_mfma_f32_32x32x16_bf16 v[128:143], v[168:171], v[184:187], v[128:143]\n"
"v_mfma_f32_32x32x16_bf16 v[144:159], v[168:171], v[188:191], v[144:159]\n"
"s_waitcnt vmcnt(21)\n"
"ds_read_b64_tr_b16 v[160:161], v14 offset:20480\n"
"ds_read_b64_tr_b16 v[162:163], v14 offset:21760\n"
"ds_read_b64_tr_b16 v[164:165], v14 offset:20544\n"
"ds_read_b64_tr_b16 v[166:167], v14 offset:21824\n"
"ds_read_b64_tr_b16 v[168:169], v14 offset:20608\n"
"ds_read_b64_tr_b16 v[170:171], v14 offset:21888\n"
"s_waitcnt lgkmcnt(6)\n"
"v_mfma_f32_32x32x16_bf16 v[64:79], v[172:175], v[192:195], v[64:79]\n"
"v_mfma_f32_32x32x16_bf16 v[80:95], v[172:175], v[196:199], v[80:95]\n"
"v_mfma_f32_32x32x16_bf16 v[96:111], v[176:179], v[192:195], v[96:111]\n"
"v_mfma_f32_32x32x16_bf16 v[112:127], v[176:179], v[196:199], v[112:127]\n"
"v_mfma_f32_32x32x16_bf16 v[128:143], v[180:183], v[192:195], v[128:143]\n"
"v_mfma_f32_32x32x16_bf16 v[144:159], v[180:183], v[196:199], v[144:159]\n"
"s_waitcnt vmcnt(9)\n"
"s_mov_b64 exec, 1\n"
"ds_add_u32 v25, v24\n"
"s_mov_b64 exec, -1\n"
"s_setprio 3\n"
"s_mov_b64 exec, 1\n"
"ds_add_u32 v25, v24\n"
"s_mov_b64 exec, -1\n"
"ds_read_b64_tr_b16 v[172:173], v14 offset:23040\n"
"ds_read_b64_tr_b16 v[174:175], v14 offset:24320\n"
"ds_read_b64_tr_b16 v[176:177], v14 offset:23104\n"
"ds_read_b64_tr_b16 v[178:179], v14 offset:24384\n"
"ds_read_b64_tr_b16 v[180:181], v14 offset:23168\n"
"ds_read_b64_tr_b16 v[182:183], v14 offset:24448\n"
"s_waitcnt lgkmcnt(6)\n"
"ds_read_b64_tr_b16 v[32:33], v14 offset:0\n"
"ds_read_b64_tr_b16 v[34:35], v14 offset:1280\n"
"ds_read_b64_tr_b16 v[36:37], v14 offset:64\n"
"ds_read_b64_tr_b16 v[38:39], v14 offset:1344\n"
"ds_read_b64_tr_b16 v[40:41], v14 offset:128\n"
"ds_read_b64_tr_b16 v[42:43], v14 offset:1408\n"
"s_waitcnt lgkmcnt(6)\n"
"ds_read_b64_tr_b16 v[44:45], v14 offset:2560\n"
"ds_read_b64_tr_b16 v[46:47], v14 offset:3840\n"
"ds_read_b64_tr_b16 v[48:49], v14 offset:2624\n"
"ds_read_b64_tr_b16 v[50:51], v14 offset:3904\n"
"ds_read_b64_tr_b16 v[52:53], v14 offset:2688\n"
"ds_read_b64_tr_b16 v[54:55], v14 offset:3968\n"
"s_waitcnt lgkmcnt(6)\n"
"s_waitcnt vmcnt(4)\n"
"ds_read_b64_tr_b16 v[184:185], v14 offset:5120\n"
"ds_read_b64_tr_b16 v[186:187], v14 offset:6400\n"
"ds_read_b64_tr_b16 v[188:189], v14 offset:5184\n"
"ds_read_b64_tr_b16 v[190:191], v14 offset:6464\n"
"ds_read_b64_tr_b16 v[192:193], v14 offset:5248\n"
"ds_read_b64_tr_b16 v[194:195], v14 offset:6528\n"
"s_waitcnt lgkmcnt(6)\n"
"ds_read_b64_tr_b16 v[56:57], v14 offset:7680\n"
"ds_read_b64_tr_b16 v[58:59], v14 offset:8960\n"
"ds_read_b64_tr_b16 v[196:197], v14 offset:7744\n"
"ds_read_b64_tr_b16 v[198:199], v14 offset:9024\n"
"ds_read_b64_tr_b16 v[248:249], v14 offset:7808\n"
"ds_read_b64_tr_b16 v[250:251], v14 offset:9088\n"
"s_waitcnt lgkmcnt(6)\n"
"s_waitcnt lgkmcnt(0)\n"
"s_mul_i32 s63, s34, 204800\n"
"s_add_u32 s63, s63, 0\n"
"s_add_u32 s61, s35, 0\n"
"s_mov_b32 m0, s61\n"
"s_nop 0\n"
"buffer_load_dwordx4 v27, s[44:47], s63 offen lds\n"
"s_add_u32 s61, s35, 1024\n"
"s_mov_b32 m0, s61\n"
"s_nop 0\n"
"buffer_load_dwordx4 v28, s[44:47], s63 offen lds\n"
"s_add_u32 s61, s35, 2048\n"
"s_mov_b32 m0, s61\n"
"s_nop 0\n"
"buffer_load_dwordx4 v29, s[44:47], s63 offen lds\n"
"s_add_u32 s61, s35, 3072\n"
"s_mov_b32 m0, s61\n"
"s_nop 0\n"
"buffer_load_dwordx4 v30, s[44:47], s63 offen lds\n"
"s_add_u32 s61, s35, 4096\n"
"s_mov_b32 m0, s61\n"
"s_nop 0\n"
"buffer_load_dwordx4 v31, s[44:47], s63 offen lds\n"
"s_mul_i32 s63, s34, 204800\n"
"s_add_u32 s63, s63, 40960\n"
"s_add_u32 s61, s35, 5120\n"
"s_mov_b32 m0, s61\n"
"s_nop 0\n"
"buffer_load_dwordx4 v27, s[44:47], s63 offen lds\n"
"s_add_u32 s61, s35, 6144\n"
"s_mov_b32 m0, s61\n"
"s_nop 0\n"
"buffer_load_dwordx4 v28, s[44:47], s63 offen lds\n"
"s_add_u32 s61, s35, 7168\n"
"s_mov_b32 m0, s61\n"
"s_nop 0\n"
"buffer_load_dwordx4 v29, s[44:47], s63 offen lds\n"
"s_add_u32 s61, s35, 8192\n"
"s_mov_b32 m0, s61\n"
"s_nop 0\n"
"buffer_load_dwordx4 v30, s[44:47], s63 offen lds\n"
"s_add_u32 s61, s35, 9216\n"
"s_mov_b32 m0, s61\n"
"s_nop 0\n"
"buffer_load_dwordx4 v31, s[44:47], s63 offen lds\n"
"s_mul_i32 s63, s34, 204800\n"
"s_add_u32 s63, s63, 81920\n"
"s_add_u32 s61, s35, 10240\n"
"s_mov_b32 m0, s61\n"
"s_nop 0\n"
"buffer_load_dwordx4 v27, s[44:47], s63 offen lds\n"
"s_add_u32 s61, s35, 11264\n"
"s_mov_b32 m0, s61\n"
"s_nop 0\n"
"buffer_load_dwordx4 v28, s[44:47], s63 offen lds\n"
"s_add_u32 s61, s35, 12288\n"
"s_mov_b32 m0, s61\n"
"s_nop 0\n"
"buffer_load_dwordx4 v29, s[44:47], s63 offen lds\n"
"s_add_u32 s61, s35, 13312\n"
"s_mov_b32 m0, s61\n"
"s_nop 0\n"
"buffer_load_dwordx4 v30, s[44:47], s63 offen lds\n"
"s_add_u32 s61, s35, 14336\n"
"s_mov_b32 m0, s61\n"
"s_nop 0\n"
"buffer_load_dwordx4 v31, s[44:47], s63 offen lds\n"
"s_mul_i32 s63, s34, 204800\n"
"s_add_u32 s63, s63, 122880\n"
"s_add_u32 s61, s35, 15360\n"
"s_mov_b32 m0, s61\n"
"s_nop 0\n"
"buffer_load_dwordx4 v27, s[44:47], s63 offen lds\n"
"s_add_u32 s61, s35, 16384\n"
"s_mov_b32 m0, s61\n"
"s_nop 0\n"
"buffer_load_dwordx4 v28, s[44:47], s63 offen lds\n"
"s_add_u32 s61, s35, 17408\n"
"s_mov_b32 m0, s61\n"
"s_nop 0\n"
"buffer_load_dwordx4 v29, s[44:47], s63 offen lds\n"
"s_add_u32 s61, s35, 18432\n"
"s_mov_b32 m0, s61\n"
"s_nop 0\n"
"buffer_load_dwordx4 v30, s[44:47], s63 offen lds\n"
"s_add_u32 s61, s35, 19456\n"
"s_mov_b32 m0, s61\n"
"s_nop 0\n"
"buffer_load_dwordx4 v31, s[44:47], s63 offen lds\n"
"s_mul_i32 s63, s34, 204800\n"
"s_add_u32 s63, s63, 163840\n"
"s_add_u32 s61, s35, 20480\n"
"s_mov_b32 m0, s61\n"
"s_nop 0\n"
"buffer_load_dwordx4 v27, s[44:47], s63 offen lds\n"
"s_add_u32 s61, s35, 21504\n"
"s_mov_b32 m0, s61\n"
"s_nop 0\n"
"buffer_load_dwordx4 v28, s[44:47], s63 offen lds\n"
"s_add_u32 s61, s35, 22528\n"
"s_mov_b32 m0, s61\n"
"s_nop 0\n"
"buffer_load_dwordx4 v29, s[44:47], s63 offen lds\n"
"s_add_u32 s61, s35, 23552\n"
"s_mov_b32 m0, s61\n"
"s_nop 0\n"
"buffer_load_dwordx4 v30, s[44:47], s63 offen lds\n"
"s_add_u32 s61, s35, 24576\n"
"s_mov_b32 m0, s61\n"
"s_nop 0\n"
"buffer_load_dwordx4 v31, s[44:47], s63 offen lds\n"
"s_waitcnt vmcnt(25)\n"
"s_setprio 0\n"
"s_mov_b32 s69, 0\n"
"L_sp42:\n"
"ds_read_b32 v26, v7\n"
"s_waitcnt lgkmcnt(0)\n"
"v_cmp_gt_u32 vcc, 16, v26\n"
"s_cbranch_vccz L_spd42\n"
"s_sleep 2\n"
"s_add_u32 s69, s69, 1\n"
"s_cmp_lt_u32 s69, 100000\n"
"s_cbranch_scc1 L_sp42\n"
"L_spd42:\n"
"s_setprio 3\n"
"v_mfma_f32_32x32x16_bf16 v[64:79], v[160:163], v[200:203], v[64:79]\n"
"v_mfma_f32_32x32x16_bf16 v[80:95], v[160:163], v[204:207], v[80:95]\n"
"v_mfma_f32_32x32x16_bf16 v[96:111], v[164:167], v[200:203], v[96:111]\n"
"v_mfma_f32_32x32x16_bf16 v[112:127], v[164:167], v[204:207], v[112:127]\n"
"v_mfma_f32_32x32x16_bf16 v[128:143], v[168:171], v[200:203], v[128:143]\n"
"v_mfma_f32_32x32x16_bf16 v[144:159], v[168:171], v[204:207], v[144:159]\n"
"v_mfma_f32_32x32x16_bf16 v[64:79], v[172:175], v[208:211], v[64:79]\n"
"v_mfma_f32_32x32x16_bf16 v[80:95], v[172:175], v[212:215], v[80:95]\n"
"v_mfma_f32_32x32x16_bf16 v[96:111], v[176:179], v[208:211], v[96:111]\n"
"v_mfma_f32_32x32x16_bf16 v[112:127], v[176:179], v[212:215], v[112:127]\n"
"v_mfma_f32_32x32x16_bf16 v[128:143], v[180:183], v[208:211], v[128:143]\n"
"v_mfma_f32_32x32x16_bf16 v[144:159], v[180:183], v[212:215], v[144:159]\n"
"v_mfma_f32_32x32x16_bf16 v[64:79], v[32:35], v[216:219], v[64:79]\n"
"v_mfma_f32_32x32x16_bf16 v[80:95], v[32:35], v[220:223], v[80:95]\n"
"v_mfma_f32_32x32x16_bf16 v[96:111], v[36:39], v[216:219], v[96:111]\n"
"v_mfma_f32_32x32x16_bf16 v[112:127], v[36:39], v[220:223], v[112:127]\n"
"v_mfma_f32_32x32x16_bf16 v[128:143], v[40:43], v[216:219], v[128:143]\n"
"v_mfma_f32_32x32x16_bf16 v[144:159], v[40:43], v[220:223], v[144:159]\n"
"v_mfma_f32_32x32x16_bf16 v[64:79], v[44:47], v[224:227], v[64:79]\n"
"v_mfma_f32_32x32x16_bf16 v[80:95], v[44:47], v[228:231], v[80:95]\n"
"v_mfma_f32_32x32x16_bf16 v[96:111], v[48:51], v[224:227], v[96:111]\n"
"v_mfma_f32_32x32x16_bf16 v[112:127], v[48:51], v[228:231], v[112:127]\n"
"v_mfma_f32_32x32x16_bf16 v[128:143], v[52:55], v[224:227], v[128:143]\n"
"v_mfma_f32_32x32x16_bf16 v[144:159], v[52:55], v[228:231], v[144:159]\n"
"v_mfma_f32_32x32x16_bf16 v[64:79], v[184:187], v[232:235], v[64:79]\n"
"v_mfma_f32_32x32x16_bf16 v[80:95], v[184:187], v[236:239], v[80:95]\n"
"v_mfma_f32_32x32x16_bf16 v[96:111], v[188:191], v[232:235], v[96:111]\n"
"v_mfma_f32_32x32x16_bf16 v[112:127], v[188:191], v[236:239], v[112:127]\n"
"v_mfma_f32_32x32x16_bf16 v[128:143], v[192:195], v[232:235], v[128:143]\n"
"v_mfma_f32_32x32x16_bf16 v[144:159], v[192:195], v[236:239], v[144:159]\n"
"v_mfma_f32_32x32x16_bf16 v[64:79], v[56:59], v[240:243], v[64:79]\n"
"v_mfma_f32_32x32x16_bf16 v[80:95], v[56:59], v[244:247], v[80:95]\n"
"v_mfma_f32_32x32x16_bf16 v[96:111], v[196:199], v[240:243], v[96:111]\n"
"v_mfma_f32_32x32x16_bf16 v[112:127], v[196:199], v[244:247], v[112:127]\n"
"v_mfma_f32_32x32x16_bf16 v[128:143], v[248:251], v[240:243], v[128:143]\n"
"v_mfma_f32_32x32x16_bf16 v[144:159], v[248:251], v[244:247], v[144:159]\n"
"s_mov_b32 s52, s24\n"
"s_and_b32 s53, s25, 0xffff\n"
"s_mov_b32 s54, 1280\n"
"s_mov_b32 s55, 0x20000\n"
"v_add_u32 v17, 0, %2\n"
"v_mul_u32_u24 v18, 6554, v17\n"
"v_lshrrev_b32 v18, 16, v18\n"
"v_mul_u32_u24 v19, 10, v18\n"
"v_sub_u32 v19, v17, v19\n"
"v_mul_u32_u24 v46, 336, v18\n"
"v_lshl_add_u32 v46, v19, 5, v46\n"
"v_add_u32 v46, 41984, v46\n"
"s_lshl_b32 s60, s30, 6\n"
"v_add_u32 v18, s60, v18\n"
"v_mul_u32_u24 v18, 1280, v18\n"
"s_mul_i32 s60, s31, 160\n"
"v_lshl_add_u32 v19, v19, 4, s60\n"
"v_add_u32 v44, v18, v19\n"
"buffer_load_dwordx4 v[48:51], v44, s[40:43], 0 offen\n"
"buffer_load_dwordx4 v[52:55], v19, s[52:55], 0 offen\n"
"v_add_u32 v17, 512, %2\n"
"v_mul_u32_u24 v18, 6554, v17\n"
"v_lshrrev_b32 v18, 16, v18\n"
"v_mul_u32_u24 v40, 10, v18\n"
"v_sub_u32 v40, v17, v40\n"
"v_mul_u32_u24 v47, 336, v18\n"
"v_lshl_add_u32 v47, v40, 5, v47\n"
"v_add_u32 v47, 41984, v47\n"
"s_lshl_b32 s60, s30, 6\n"
"v_add_u32 v18, s60, v18\n"
"v_mul_u32_u24 v18, 1280, v18\n"
"s_mul_i32 s60, s31, 160\n"
"v_lshl_add_u32 v40, v40, 4, s60\n"
"v_add_u32 v45, v18, v40\n"
"buffer_load_dwordx4 v[56:59], v45, s[40:43], 0 offen\n"
"buffer_load_dwordx4 v[60:63], v40, s[52:55], 0 offen\n"
"s_lshl_b32 s60, s30, 3\n"
"s_lshl_b32 s61, s34, 1\n"
"s_add_u32 s60, s60, s61\n"
"s_mul_i32 s71, s60, 10240\n"
"s_add_u32 s71, s71, 524288\n"
"s_waitcnt vmcnt(0)\n"
"v_lshlrev_b32 v20, 16, v52\n"
"v_and_b32 v21, 0xffff0000, v52\n"
"v_lshlrev_b32 v22, 16, v53\n"
"v_and_b32 v23, 0xffff0000, v53\n"
"v_lshlrev_b32 v24, 16, v54\n"
"v_and_b32 v25, 0xffff0000, v54\n"
"v_lshlrev_b32 v26, 16, v55\n"
"v_and_b32 v27, 0xffff0000, v55\n"
"v_and_b32 v28, 0xffff0000, v51\n"
"v_lshlrev_b32 v29, 16, v51\n"
"v_add_f32 v55, v28, v27\n"
"v_add_f32 v54, v29, v26\n"
"v_and_b32 v28, 0xffff0000, v50\n"
"v_lshlrev_b32 v29, 16, v50\n"
"v_add_f32 v53, v28, v25\n"
"v_add_f32 v52, v29, v24\n"
"v_and_b32 v28, 0xffff0000, v49\n"
"v_lshlrev_b32 v29, 16, v49\n"
"v_add_f32 v51, v28, v23\n"
"v_add_f32 v50, v29, v22\n"
"v_and_b32 v28, 0xffff0000, v48\n"
"v_lshlrev_b32 v29, 16, v48\n"
"v_add_f32 v49, v28, v21\n"
"v_add_f32 v48, v29, v20\n"
"v_lshlrev_b32 v20, 16, v60\n"
"v_and_b32 v21, 0xffff0000, v60\n"
"v_lshlrev_b32 v22, 16, v61\n"
"v_and_b32 v23, 0xffff0000, v61\n"
"v_lshlrev_b32 v24, 16, v62\n"
"v_and_b32 v25, 0xffff0000, v62\n"
"v_lshlrev_b32 v26, 16, v63\n"
"v_and_b32 v27, 0xffff0000, v63\n"
"v_and_b32 v28, 0xffff0000, v59\n"
"v_lshlrev_b32 v29, 16, v59\n"
"v_add_f32 v63, v28, v27\n"
"v_add_f32 v62, v29, v26\n"
"v_and_b32 v28, 0xffff0000, v58\n"
"v_lshlrev_b32 v29, 16, v58\n"
"v_add_f32 v61, v28, v25\n"
"v_add_f32 v60, v29, v24\n"
"v_and_b32 v28, 0xffff0000, v57\n"
"v_lshlrev_b32 v29, 16, v57\n"
"v_add_f32 v59, v28, v23\n"
"v_add_f32 v58, v29, v22\n"
"v_and_b32 v28, 0xffff0000, v56\n"
"v_lshlrev_b32 v29, 16, v56\n"
"v_add_f32 v57, v28, v21\n"
"v_add_f32 v56, v29, v20\n"
"s_add_u32 s61, s71, 0\n"
"buffer_load_dwordx4 v[168:171], v15, s[36:39], s61 offen sc1\n"
"s_add_u32 s61, s71, 5120\n"
"buffer_load_dwordx4 v[172:175], v15, s[36:39], s61 offen sc1\n"
"s_add_u32 s61, s71, 1024\n"
"buffer_load_dwordx4 v[176:179], v15, s[36:39], s61 offen sc1\n"
"s_add_u32 s61, s71, 6144\n"
"buffer_load_dwordx4 v[180:183], v15, s[36:39], s61 offen sc1\n"
"s_add_u32 s61, s71, 2048\n"
"buffer_load_dwordx4 v[184:187], v15, s[36:39], s61 offen sc1\n"
"s_add_u32 s61, s71, 7168\n"
"buffer_load_dwordx4 v[188:191], v15, s[36:39], s61 offen sc1\n"
"s_add_u32 s61, s71, 3072\n"
"buffer_load_dwordx4 v[192:195], v15, s[36:39], s61 offen sc1\n"
"s_add_u32 s61, s71, 8192\n"
"buffer_load_dwordx4 v[196:199], v15, s[36:39], s61 offen sc1\n"
"s_add_u32 s61, s71, 4096\n"
"buffer_load_dwordx4 v[200:203], v15, s[36:39], s61 offen sc1\n"
"s_add_u32 s61, s71, 9216\n"
"buffer_load_dwordx4 v[204:207], v15, s[36:39], s61 offen sc1\n"
"ds_read_b64_tr_b16 v[20:21], v14 offset:0\n"
"ds_read_b64_tr_b16 v[22:23], v14 offset:1280\n"
"ds_read_b64_tr_b16 v[24:25], v14 offset:64\n"
"ds_read_b64_tr_b16 v[26:27], v14 offset:1344\n"
"ds_read_b64_tr_b16 v[28:29], v14 offset:128\n"
"ds_read_b64_tr_b16 v[30:31], v14 offset:1408\n"
"s_mov_b32 s69, 0\n"
"L_oc43:\n"
"s_waitcnt vmcnt(0)\n"
"v_max3_u32 v16, v168, v169, v170\n"
"v_max3_u32 v16, v16, v171, v172\n"
"v_max3_u32 v16, v16, v173, v174\n"
"v_max3_u32 v16, v16, v175, v176\n"
"v_max3_u32 v16, v16, v177, v178\n"
"v_max3_u32 v16, v16, v179, v180\n"
"v_max3_u32 v16, v16, v181, v182\n"
"v_max3_u32 v16, v16, v183, v184\n"
"v_max3_u32 v16, v16, v185, v186\n"
"v_max3_u32 v16, v16, v187, v188\n"
"v_max3_u32 v16, v16, v189, v190\n"
"v_max3_u32 v16, v16, v191, v192\n"
"v_max3_u32 v16, v16, v193, v194\n"
"v_max3_u32 v16, v16, v195, v196\n"
"v_max3_u32 v16, v16, v197, v198\n"
"v_max3_u32 v16, v16, v199, v200\n"
"v_max3_u32 v16, v16, v201, v202\n"
"v_max3_u32 v16, v16, v203, v204\n"
"v_max3_u32 v16, v16, v205, v206\n"
"v_max_u32 v16, v16, v207\n"
"v_cmp_eq_u32 vcc, -1, v16\n"
"s_cbranch_vccz L_ocd43\n"
"s_add_u32 s61, s71, 0\n"
"buffer_load_dwordx4 v[168:171], v15, s[36:39], s61 offen sc1\n"
"s_add_u32 s61, s71, 5120\n"
"buffer_load_dwordx4 v[172:175], v15, s[36:39], s61 offen sc1\n"
"s_add_u32 s61, s71, 1024\n"
"buffer_load_dwordx4 v[176:179], v15, s[36:39], s61 offen sc1\n"
"s_add_u32 s61, s71, 6144\n"
"buffer_load_dwordx4 v[180:183], v15, s[36:39], s61 offen sc1\n"
"s_add_u32 s61, s71, 2048\n"
"buffer_load_dwordx4 v[184:187], v15, s[36:39], s61 offen sc1\n"
"s_add_u32 s61, s71, 7168\n"
"buffer_load_dwordx4 v[188:191], v15, s[36:39], s61 offen sc1\n"
"s_add_u32 s61, s71, 3072\n"
"buffer_load_dwordx4 v[192:195], v15, s[36:39], s61 offen sc1\n"
"s_add_u32 s61, s71, 8192\n"
"buffer_load_dwordx4 v[196:199], v15, s[36:39], s61 offen sc1\n"
"s_add_u32 s61, s71, 4096\n"
"buffer_load_dwordx4 v[200:203], v15, s[36:39], s61 offen sc1\n"
"s_add_u32 s61, s71, 9216\n"
"buffer_load_dwordx4 v[204:207], v15, s[36:39], s61 offen sc1\n"
"s_add_u32 s69, s69, 1\n"
"s_cmp_lt_u32 s69, 100000\n"
"s_cbranch_scc1 L_oc43\n"
"s_waitcnt vmcnt(0)\n"
"L_ocd43:\n"
"s_add_u32 s61, s71, 10240\n"
"buffer_load_dwordx4 v[208:211], v15, s[36:39], s61 offen sc1\n"
"s_add_u32 s61, s71, 15360\n"
"buffer_load_dwordx4 v[212:215], v15, s[36:39], s61 offen sc1\n"
"s_add_u32 s61, s71, 11264\n"
"buffer_load_dwordx4 v[216:219], v15, s[36:39], s61 offen sc1\n"
"s_add_u32 s61, s71, 16384\n"
"buffer_load_dwordx4 v[220:223], v15, s[36:39], s61 offen sc1\n"
"s_add_u32 s61, s71, 12288\n"
"buffer_load_dwordx4 v[224:227], v15, s[36:39], s61 offen sc1\n"
"s_add_u32 s61, s71, 17408\n"
"buffer_load_dwordx4 v[228:231], v15, s[36:39], s61 offen sc1\n"
"s_add_u32 s61, s71, 13312\n"
"buffer_load_dwordx4 v[232:235], v15, s[36:39], s61 offen sc1\n"
"s_add_u32 s61, s71, 18432\n"
"buffer_load_dwordx4 v[236:239], v15, s[36:39], s61 offen sc1\n"
"s_add_u32 s61, s71, 14336\n"
"buffer_load_dwordx4 v[240:243], v15, s[36:39], s61 offen sc1\n"
"s_add_u32 s61, s71, 19456\n"
"buffer_load_dwordx4 v[244:247], v15, s[36:39], s61 offen sc1\n"
"ds_read_b64_tr_b16 v[32:33], v14 offset:3200\n"
"ds_read_b64_tr_b16 v[34:35], v14 offset:4480\n"
"ds_read_b64_tr_b16 v[36:37], v14 offset:3264\n"
"ds_read_b64_tr_b16 v[38:39], v14 offset:4544\n"
"ds_read_b64_tr_b16 v[40:41], v14 offset:3328\n"
"ds_read_b64_tr_b16 v[42:43], v14 offset:4608\n"
"s_waitcnt lgkmcnt(6)\n"
"v_mfma_f32_32x32x16_bf16 v[64:79], v[20:23], v[168:171], v[64:79]\n"
"v_mfma_f32_32x32x16_bf16 v[80:95], v[20:23], v[172:175], v[80:95]\n"
"v_mfma_f32_32x32x16_bf16 v[96:111], v[24:27], v[168:171], v[96:111]\n"
"v_mfma_f32_32x32x16_bf16 v[112:127], v[24:27], v[172:175], v[112:127]\n"
"v_mfma_f32_32x32x16_bf16 v[128:143], v[28:31], v[168:171], v[128:143]\n"
"v_mfma_f32_32x32x16_bf16 v[144:159], v[28:31], v[172:175], v[144:159]\n"
"ds_read_b64_tr_b16 v[20:21], v14 offset:6400\n"
"ds_read_b64_tr_b16 v[22:23], v14 offset:7680\n"
"ds_read_b64_tr_b16 v[24:25], v14 offset:6464\n"
"ds_read_b64_tr_b16 v[26:27], v14 offset:7744\n"
"ds_read_b64_tr_b16 v[28:29], v14 offset:6528\n"
"ds_read_b64_tr_b16 v[30:31], v14 offset:7808\n"
"s_waitcnt lgkmcnt(6)\n"
"v_mfma_f32_32x32x16_bf16 v[64:79], v[32:35], v[176:179], v[64:79]\n"
"v_mfma_f32_32x32x16_bf16 v[80:95], v[32:35], v[180:183], v[80:95]\n"
"v_mfma_f32_32x32x16_bf16 v[96:111], v[36:39], v[176:179], v[96:111]\n"
"v_mfma_f32_32x32x16_bf16 v[112:127], v[36:39], v[180:183], v[112:127]\n"
"v_mfma_f32_32x32x16_bf16 v[128:143], v[40:43], v[176:179], v[128:143]\n"
"v_mfma_f32_32x32x16_bf16 v[144:159], v[40:43], v[180:183], v[144:159]\n"
"ds_read_b64_tr_b16 v[32:33], v14 offset:9600\n"
"ds_read_b64_tr_b16 v[34:35], v14 offset:10880\n"
"ds_read_b64_tr_b16 v[36:37], v14 offset:9664\n"
"ds_read_b64_tr_b16 v[38:39], v14 offset:10944\n"
"ds_read_b64_tr_b16 v[40:41], v14 offset:9728\n"
"ds_read_b64_tr_b16 v[42:43], v14 offset:11008\n"
"s_waitcnt lgkmcnt(6)\n"
"v_mfma_f32_32x32x16_bf16 v[64:79], v[20:23], v[184:187], v[64:79]\n"
"v_mfma_f32_32x32x16_bf16 v[80:95], v[20:23], v[188:191], v[80:95]\n"
"v_mfma_f32_32x32x16_bf16 v[96:111], v[24:27], v[184:187], v[96:111]\n"
"v_mfma_f32_32x32x16_bf16 v[112:127], v[24:27], v[188:191], v[112:127]\n"
"v_mfma_f32_32x32x16_bf16 v[128:143], v[28:31], v[184:187], v[128:143]\n"
"v_mfma_f32_32x32x16_bf16 v[144:159], v[28:31], v[188:191], v[144:159]\n"
"ds_read_b64_tr_b16 v[20:21], v13 offset:2560\n"
"ds_read_b64_tr_b16 v[22:23], v13 offset:5760\n"
"ds_read_b64_tr_b16 v[24:25], v13 offset:2624\n"
"ds_read_b64_tr_b16 v[26:27], v13 offset:5824\n"
"ds_read_b64_tr_b16 v[28:29], v13 offset:2688\n"
"ds_read_b64_tr_b16 v[30:31], v13 offset:5888\n"
"s_waitcnt lgkmcnt(6)\n"
"v_mfma_f32_32x32x16_bf16 v[64:79], v[32:35], v[192:195], v[64:79]\n"
"v_mfma_f32_32x32x16_bf16 v[80:95], v[32:35], v[196:199], v[80:95]\n"
"v_mfma_f32_32x32x16_bf16 v[96:111], v[36:39], v[192:195], v[96:111]\n"
"v_mfma_f32_32x32x16_bf16 v[112:127], v[36:39], v[196:199], v[112:127]\n"
"v_mfma_f32_32x32x16_bf16 v[128:143], v[40:43], v[192:195], v[128:143]\n"
"v_mfma_f32_32x32x16_bf16 v[144:159], v[40:43], v[196:199], v[144:159]\n"
"ds_read_b64_tr_b16 v[32:33], v14 offset:12800\n"
"ds_read_b64_tr_b16 v[34:35], v14 offset:14080\n"
"ds_read_b64_tr_b16 v[36:37], v14 offset:12864\n"
"ds_read_b64_tr_b16 v[38:39], v14 offset:14144\n"
"ds_read_b64_tr_b16 v[40:41], v14 offset:12928\n"
"ds_read_b64_tr_b16 v[42:43], v14 offset:14208\n"
"s_waitcnt lgkmcnt(6)\n"
"v_mfma_f32_32x32x16_bf16 v[64:79], v[20:23], v[200:203], v[64:79]\n"
"v_mfma_f32_32x32x16_bf16 v[80:95], v[20:23], v[204:207], v[80:95]\n"
"v_mfma_f32_32x32x16_bf16 v[96:111], v[24:27], v[200:203], v[96:111]\n"
"v_mfma_f32_32x32x16_bf16 v[112:127], v[24:27], v[204:207], v[112:127]\n"
"v_mfma_f32_32x32x16_bf16 v[128:143], v[28:31], v[200:203], v[128:143]\n"
"v_mfma_f32_32x32x16_bf16 v[144:159], v[28:31], v[204:207], v[144:159]\n"
"s_mov_b32 s69, 0\n"
"L_oc44:\n"
"s_waitcnt vmcnt(0)\n"
"v_max3_u32 v16, v208, v209, v210\n"
"v_max3_u32 v16, v16, v211, v212\n"
"v_max3_u32 v16, v16, v213, v214\n"
"v_max3_u32 v16, v16, v215, v216\n"
"v_max3_u32 v16, v16, v217, v218\n"
"v_max3_u32 v16, v16, v219, v220\n"
"v_max3_u32 v16, v16, v221, v222\n"
"v_max3_u32 v16, v16, v223, v224\n"
"v_max3_u32 v16, v16, v225, v226\n"
"v_max3_u32 v16, v16, v227, v228\n"
"v_max3_u32 v16, v16, v229, v230\n"
"v_max3_u32 v16, v16, v231, v232\n"
"v_max3_u32 v16, v16, v233, v234\n"
"v_max3_u32 v16, v16, v235, v236\n"
"v_max3_u32 v16, v16, v237, v238\n"
"v_max3_u32 v16, v16, v239, v240\n"
"v_max3_u32 v16, v16, v241, v242\n"
"v_max3_u32 v16, v16, v243, v244\n"
"v_max3_u32 v16, v16, v245, v246\n"
"v_max_u32 v16, v16, v247\n"
"v_cmp_eq_u32 vcc, -1, v16\n"
"s_cbranch_vccz L_ocd44\n"
"s_add_u32 s61, s71, 10240\n"
"buffer_load_dwordx4 v[208:211], v15, s[36:39], s61 offen sc1\n"
"s_add_u32 s61, s71, 15360\n"
"buffer_load_dwordx4 v[212:215], v15, s[36:39], s61 offen sc1\n"
"s_add_u32 s61, s71, 11264\n"
"buffer_load_dwordx4 v[216:219], v15, s[36:39], s61 offen sc1\n"
"s_add_u32 s61, s71, 16384\n"
"buffer_load_dwordx4 v[220:223], v15, s[36:39], s61 offen sc1\n"
"s_add_u32 s61, s71, 12288\n"
"buffer_load_dwordx4 v[224:227], v15, s[36:39], s61 offen sc1\n"
"s_add_u32 s61, s71, 17408\n"
"buffer_load_dwordx4 v[228:231], v15, s[36:39], s61 offen sc1\n"
"s_add_u32 s61, s71, 13312\n"
"buffer_load_dwordx4 v[232:235], v15, s[36:39], s61 offen sc1\n"
"s_add_u32 s61, s71, 18432\n"
"buffer_load_dwordx4 v[236:239], v15, s[36:39], s61 offen sc1\n"
"s_add_u32 s61, s71, 14336\n"
"buffer_load_dwordx4 v[240:243], v15, s[36:39], s61 offen sc1\n"
"s_add_u32 s61, s71, 19456\n"
"buffer_load_dwordx4 v[244:247], v15, s[36:39], s61 offen sc1\n"
"s_add_u32 s69, s69, 1\n"
"s_cmp_lt_u32 s69, 100000\n"
"s_cbranch_scc1 L_oc44\n"
"s_waitcnt vmcnt(0)\n"
"L_ocd44:\n"
"ds_read_b64_tr_b16 v[20:21], v14 offset:16000\n"
"ds_read_b64_tr_b16 v[22:23], v14 offset:17280\n"
"ds_read_b64_tr_b16 v[24:25], v14 offset:16064\n"
"ds_read_b64_tr_b16 v[26:27], v14 offset:17344\n"
"ds_read_b64_tr_b16 v[28:29], v14 offset:16128\n"
"ds_read_b64_tr_b16 v[30:31], v14 offset:17408\n"
"s_waitcnt lgkmcnt(6)\n"
"v_mfma_f32_32x32x16_bf16 v[64:79], v[32:35], v[208:211], v[64:79]\n"
"v_mfma_f32_32x32x16_bf16 v[80:95], v[32:35], v[212:215], v[80:95]\n"
"v_mfma_f32_32x32x16_bf16 v[96:111], v[36:39], v[208:211], v[96:111]\n"
"v_mfma_f32_32x32x16_bf16 v[112:127], v[36:39], v[212:215], v[112:127]\n"
"v_mfma_f32_32x32x16_bf16 v[128:143], v[40:43], v[208:211], v[128:143]\n"
"v_mfma_f32_32x32x16_bf16 v[144:159], v[40:43], v[212:215], v[144:159]\n"
"ds_read_b64_tr_b16 v[32:33], v14 offset:19200\n"
"ds_read_b64_tr_b16 v[34:35], v14 offset:20480\n"
"ds_read_b64_tr_b16 v[36:37], v14 offset:19264\n"
"ds_read_b64_tr_b16 v[38:39], v14 offset:20544\n"
"ds_read_b64_tr_b16 v[40:41], v14 offset:19328\n"
"ds_read_b64_tr_b16 v[42:43], v14 offset:20608\n"
"s_waitcnt lgkmcnt(6)\n"
"v_mfma_f32_32x32x16_bf16 v[64:79], v[20:23], v[216:219], v[64:79]\n"
"v_mfma_f32_32x32x16_bf16 v[80:95], v[20:23], v[220:223], v[80:95]\n"
"v_mfma_f32_32x32x16_bf16 v[96:111], v[24:27], v[216:219], v[96:111]\n"
"v_mfma_f32_32x32x16_bf16 v[112:127], v[24:27], v[220:223], v[112:127]\n"
"v_mfma_f32_32x32x16_bf16 v[128:143], v[28:31], v[216:219], v[128:143]\n"
"v_mfma_f32_32x32x16_bf16 v[144:159], v[28:31], v[220:223], v[144:159]\n"
"ds_read_b64_tr_b16 v[20:21], v14 offset:22400\n"
"ds_read_b64_tr_b16 v[22:23], v14 offset:23680\n"
"ds_read_b64_tr_b16 v[24:25], v14 offset:22464\n"
"ds_read_b64_tr_b16 v[26:27], v14 offset:23744\n"
"ds_read_b64_tr_b16 v[28:29], v14 offset:22528\n"
"ds_read_b64_tr_b16 v[30:31], v14 offset:23808\n"
"s_waitcnt lgkmcnt(6)\n"
"v_mfma_f32_32x32x16_bf16 v[64:79], v[32:35], v[224:227], v[64:79]\n"
"v_mfma_f32_32x32x16_bf16 v[80:95], v[32:35], v[228:231], v[80:95]\n"
"v_mfma_f32_32x32x16_bf16 v[96:111], v[36:39], v[224:227], v[96:111]\n"
"v_mfma_f32_32x32x16_bf16 v[112:127], v[36:39], v[228:231], v[112:127]\n"
"v_mfma_f32_32x32x16_bf16 v[128:143], v[40:43], v[224:227], v[128:143]\n"
"v_mfma_f32_32x32x16_bf16 v[144:159], v[40:43], v[228:231], v[144:159]\n"
"ds_read_b64_tr_b16 v[32:33], v13 offset:15360\n"
"ds_read_b64_tr_b16 v[34:35], v13 offset:18560\n"
"ds_read_b64_tr_b16 v[36:37], v13 offset:15424\n"
"ds_read_b64_tr_b16 v[38:39], v13 offset:18624\n"
"ds_read_b64_tr_b16 v[40:41], v13 offset:15488\n"
"ds_read_b64_tr_b16 v[42:43], v13 offset:18688\n"
"s_waitcnt lgkmcnt(6)\n"
"v_mfma_f32_32x32x16_bf16 v[64:79], v[20:23], v[232:235], v[64:79]\n"
"v_mfma_f32_32x32x16_bf16 v[80:95], v[20:23], v[236:239], v[80:95]\n"
"v_mfma_f32_32x32x16_bf16 v[96:111], v[24:27], v[232:235], v[96:111]\n"
"v_mfma_f32_32x32x16_bf16 v[112:127], v[24:27], v[236:239], v[112:127]\n"
"v_mfma_f32_32x32x16_bf16 v[128:143], v[28:31], v[232:235], v[128:143]\n"
"v_mfma_f32_32x32x16_bf16 v[144:159], v[28:31], v[236:239], v[144:159]\n"
"s_waitcnt lgkmcnt(0)\n"
"v_mfma_f32_32x32x16_bf16 v[64:79], v[32:35], v[240:243], v[64:79]\n"
"v_mfma_f32_32x32x16_bf16 v[80:95], v[32:35], v[244:247], v[80:95]\n"
"v_mfma_f32_32x32x16_bf16 v[96:111], v[36:39], v[240:243], v[96:111]\n"
"v_mfma_f32_32x32x16_bf16 v[112:127], v[36:39], v[244:247], v[112:127]\n"
"v_mfma_f32_32x32x16_bf16 v[128:143], v[40:43], v[240:243], v[128:143]\n"
"v_mfma_f32_32x32x16_bf16 v[144:159], v[40:43], v[244:247], v[144:159]\n"
"v_mul_u32_u24 v16, 336, v2\n"
"v_lshl_add_u32 v16, v3, 4, v16\n"
"v_add_u32 v16, s35, v16\n"
"s_nop 3\n"
"ds_write_b128 v16, v[64:67] offset:0\n"
"ds_write_b128 v16, v[68:71] offset:32\n"
"ds_write_b128 v16, v[72:75] offset:64\n"
"ds_write_b128 v16, v[76:79] offset:96\n"
"ds_write_b128 v16, v[80:83] offset:10752\n"
"ds_write_b128 v16, v[84:87] offset:10784\n"
"ds_write_b128 v16, v[88:91] offset:10816\n"
"ds_write_b128 v16, v[92:95] offset:10848\n"
"ds_write_b128 v16, v[96:99] offset:128\n"
"ds_write_b128 v16, v[100:103] offset:160\n"
"ds_write_b128 v16, v[104:107] offset:192\n"
"ds_write_b128 v16, v[108:111] offset:224\n"
"s_waitcnt lgkmcnt(4)\n"
"ds_write_b128 v16, v[112:115] offset:10880\n"
"ds_write_b128 v16, v[116:119] offset:10912\n"
"ds_write_b128 v16, v[120:123] offset:10944\n"
"ds_write_b128 v16, v[124:127] offset:10976\n"
"ds_write_b128 v16, v[128:131] offset:256\n"
"ds_write_b128 v16, v[132:135] offset:288\n"
"ds_write_b128 v16, v[144:147] offset:11008\n"
"ds_write_b128 v16, v[148:151] offset:11040\n"
"s_waitcnt lgkmcnt(0)\n"
"L_final:\n"
"s_barrier\n"
"s_mov_b32 s56, s26\n"
"s_and_b32 s57, s27, 0xffff\n"
"s_mov_b32 s58, 2621440\n"
"s_mov_b32 s59, 0x20000\n"
"s_cmp_ge_u32 s7, 2\n"
"s_cbranch_scc1 L_fin1\n"
"v_add_u32 v104, 59904, v46\n"
"ds_read_b128 v[64:67], v46 offset:0\n"
"ds_read_b128 v[68:71], v46 offset:16\n"
"ds_read_b128 v[72:75], v46 offset:29952\n"
"ds_read_b128 v[76:79], v46 offset:29968\n"
"ds_read_b128 v[80:83], v104 offset:0\n"
"ds_read_b128 v[84:87], v104 offset:16\n"
"ds_read_b128 v[88:91], v104 offset:29952\n"
"ds_read_b128 v[92:95], v104 offset:29968\n"
"s_waitcnt lgkmcnt(4)\n"
"v_add_u32 v105, 59904, v47\n"
"ds_read_b128 v[112:115], v47 offset:0\n"
"ds_read_b128 v[116:119], v47 offset:16\n"
"ds_read_b128 v[120:123], v47 offset:29952\n"
"ds_read_b128 v[124:127], v47 offset:29968\n"
"ds_read_b128 v[128:131], v105 offset:0\n"
"ds_read_b128 v[132:135], v105 offset:16\n"
"ds_read_b128 v[136:139], v105 offset:29952\n"
"ds_read_b128 v[140:143], v105 offset:29968\n"
"s_waitcnt lgkmcnt(8) vmcnt(0)\n"
"v_add_f32 v64, v64, v72\n"
"v_add_f32 v80, v80, v88\n"
"v_add_f32 v65, v65, v73\n"
"v_add_f32 v81, v81, v89\n"
"v_add_f32 v66, v66, v74\n"
"v_add_f32 v82, v82, v90\n"
"v_add_f32 v67, v67, v75\n"
"v_add_f32 v83, v83, v91\n"
"v_add_f32 v68, v68, v76\n"
"v_add_f32 v84, v84, v92\n"
"v_add_f32 v69, v69, v77\n"
"v_add_f32 v85, v85, v93\n"
"v_add_f32 v70, v70, v78\n"
"v_add_f32 v86, v86, v94\n"
"v_add_f32 v71, v71, v79\n"
"v_add_f32 v87, v87, v95\n"
"v_add_f32 v64, v64, v80\n"
"v_add_f32 v65, v65, v81\n"
"v_add_f32 v66, v66, v82\n"
"v_add_f32 v67, v67, v83\n"
"v_add_f32 v68, v68, v84\n"
"v_add_f32 v69, v69, v85\n"
"v_add_f32 v70, v70, v86\n"
"v_add_f32 v71, v71, v87\n"
"v_add_f32 v64, v64, v48\n"
"v_add_f32 v65, v65, v49\n"
"v_add_f32 v66, v66, v50\n"
"v_add_f32 v67, v67, v51\n"
"v_add_f32 v68, v68, v52\n"
"v_add_f32 v69, v69, v53\n"
"v_add_f32 v70, v70, v54\n"
"v_add_f32 v71, v71, v55\n"
"v_cvt_pk_bf16_f32 v100, v64, v65\n"
"v_cvt_pk_bf16_f32 v101, v66, v67\n"
"v_cvt_pk_bf16_f32 v102, v68, v69\n"
"v_cvt_pk_bf16_f32 v103, v70, v71\n"
"buffer_store_dwordx4 v[100:103], v44, s[56:59], 0 offen nt\n"
"s_waitcnt lgkmcnt(0)\n"
"v_add_f32 v112, v112, v120\n"
"v_add_f32 v128, v128, v136\n"
"v_add_f32 v113, v113, v121\n"
"v_add_f32 v129, v129, v137\n"
"v_add_f32 v114, v114, v122\n"
"v_add_f32 v130, v130, v138\n"
"v_add_f32 v115, v115, v123\n"
"v_add_f32 v131, v131, v139\n"
"v_add_f32 v116, v116, v124\n"
"v_add_f32 v132, v132, v140\n"
"v_add_f32 v117, v117, v125\n"
"v_add_f32 v133, v133, v141\n"
"v_add_f32 v118, v118, v126\n"
"v_add_f32 v134, v134, v142\n"
"v_add_f32 v119, v119, v127\n"
"v_add_f32 v135, v135, v143\n"
"v_add_f32 v112, v112, v128\n"
"v_add_f32 v113, v113, v129\n"
"v_add_f32 v114, v114, v130\n"
"v_add_f32 v115, v115, v131\n"
"v_add_f32 v116, v116, v132\n"
"v_add_f32 v117, v117, v133\n"
"v_add_f32 v118, v118, v134\n"
"v_add_f32 v119, v119, v135\n"
"v_add_f32 v112, v112, v56\n"
"v_add_f32 v113, v113, v57\n"
"v_add_f32 v114, v114, v58\n"
"v_add_f32 v115, v115, v59\n"
"v_add_f32 v116, v116, v60\n"
"v_add_f32 v117, v117, v61\n"
"v_add_f32 v118, v118, v62\n"
"v_add_f32 v119, v119, v63\n"
"v_cvt_pk_bf16_f32 v106, v112, v113\n"
"v_cvt_pk_bf16_f32 v107, v114, v115\n"
"v_cvt_pk_bf16_f32 v108, v116, v117\n"
"v_cvt_pk_bf16_f32 v109, v118, v119\n"
"buffer_store_dwordx4 v[106:109], v45, s[56:59], 0 offen nt\n"
"s_branch L_fin2\n"
"L_fin1:\n"
"v_add_u32 v104, 59904, v46\n"
"ds_read_b128 v[64:67], v46 offset:0\n"
"ds_read_b128 v[68:71], v46 offset:16\n"
"ds_read_b128 v[72:75], v46 offset:29952\n"
"ds_read_b128 v[76:79], v46 offset:29968\n"
"ds_read_b128 v[80:83], v104 offset:0\n"
"ds_read_b128 v[84:87], v104 offset:16\n"
"ds_read_b128 v[88:91], v104 offset:29952\n"
"ds_read_b128 v[92:95], v104 offset:29968\n"
"s_waitcnt lgkmcnt(0) vmcnt(0)\n"
"v_add_f32 v64, v64, v72\n"
"v_add_f32 v80, v80, v88\n"
"v_add_f32 v65, v65, v73\n"
"v_add_f32 v81, v81, v89\n"
"v_add_f32 v66, v66, v74\n"
"v_add_f32 v82, v82, v90\n"
"v_add_f32 v67, v67, v75\n"
"v_add_f32 v83, v83, v91\n"
"v_add_f32 v68, v68, v76\n"
"v_add_f32 v84, v84, v92\n"
"v_add_f32 v69, v69, v77\n"
"v_add_f32 v85, v85, v93\n"
"v_add_f32 v70, v70, v78\n"
"v_add_f32 v86, v86, v94\n"
"v_add_f32 v71, v71, v79\n"
"v_add_f32 v87, v87, v95\n"
"v_add_f32 v64, v64, v80\n"
"v_add_f32 v65, v65, v81\n"
"v_add_f32 v66, v66, v82\n"
"v_add_f32 v67, v67, v83\n"
"v_add_f32 v68, v68, v84\n"
"v_add_f32 v69, v69, v85\n"
"v_add_f32 v70, v70, v86\n"
"v_add_f32 v71, v71, v87\n"
"v_add_f32 v64, v64, v48\n"
"v_add_f32 v65, v65, v49\n"
"v_add_f32 v66, v66, v50\n"
"v_add_f32 v67, v67, v51\n"
"v_add_f32 v68, v68, v52\n"
"v_add_f32 v69, v69, v53\n"
"v_add_f32 v70, v70, v54\n"
"v_add_f32 v71, v71, v55\n"
"v_cvt_pk_bf16_f32 v100, v64, v65\n"
"v_cvt_pk_bf16_f32 v101, v66, v67\n"
"v_cvt_pk_bf16_f32 v102, v68, v69\n"
"v_cvt_pk_bf16_f32 v103, v70, v71\n"
"buffer_store_dwordx4 v[100:103], v44, s[56:59], 0 offen nt\n"
"L_fin2:\n"
"L_end:\n"
"s_waitcnt vmcnt(0)\n"
"s_endpgm\n"
  :: "s"(__builtin_amdgcn_kernarg_segment_ptr()), "s"(blockIdx.x), "v"(threadIdx.x)
  : "v1","v2","v3","v4","v5","v6","v7","v8","v9","v10","v11","v12","v13","v14","v15","v16","v17","v18","v19","v20","v21","v22","v23","v24","v25","v26","v27","v28","v29","v30","v31","v32","v33","v34","v35","v36","v37","v38","v39","v40","v41","v42","v43","v44","v45","v46","v47","v48","v49","v50","v51","v52","v53","v54","v55","v56","v57","v58","v59","v60","v61","v62","v63","v64","v65","v66","v67","v68","v69","v70","v71","v72","v73","v74","v75","v76","v77","v78","v79","v80","v81","v82","v83","v84","v85","v86","v87","v88","v89","v90","v91","v92","v93","v94","v95","v96","v97","v98","v99","v100","v101","v102","v103","v104","v105","v106","v107","v108","v109","v110","v111","v112","v113","v114","v115","v116","v117","v118","v119","v120","v121","v122","v123","v124","v125","v126","v127","v128","v129","v130","v131","v132","v133","v134","v135","v136","v137","v138","v139","v140","v141","v142","v143","v144","v145","v146","v147","v148","v149","v150","v151","v152","v153","v154","v155","v156","v157","v158","v159","v160","v161","v162","v163","v164","v165","v166","v167","v168","v169","v170","v171","v172","v173","v174","v175","v176","v177","v178","v179","v180","v181","v182","v183","v184","v185","v186","v187","v188","v189","v190","v191","v192","v193","v194","v195","v196","v197","v198","v199","v200","v201","v202","v203","v204","v205","v206","v207","v208","v209","v210","v211","v212","v213","v214","v215","v216","v217","v218","v219","v220","v221","v222","v223","v224","v225","v226","v227","v228","v229","v230","v231","v232","v233","v234","v235","v236","v237","v238","v239","v240","v241","v242","v243","v244","v245","v246","v247","v248","v249","v250","v251","v252","v253","v254","v255", "s4","s5","s6","s7","s8","s9","s10","s11","s12","s13","s14","s15","s16","s17","s18","s19","s20","s21","s22","s23","s24","s25","s26","s27","s28","s29","s30","s31","s32","s33","s34","s35","s36","s37","s38","s39","s40","s41","s42","s43","s44","s45","s46","s47","s48","s49","s50","s51","s52","s53","s54","s55","s56","s57","s58","s59","s60","s61","s62","s63","s64","s65","s66","s67","s68","s69","s70","s71","s72","s73","s74","s75","s76","s77","s78","s79","s80","s81","s82","s83","s84","s85","s86","s87","s88","s89","s90","s91","s92","s93","s94","s95","s96","s97","s98","s99", "vcc", "scc", "memory");
}


extern "C" void kernel_launch(void* const* d_in, const int* in_sizes, int n_in, void* d_out, int out_size, void* d_ws, size_t ws_size,
                              hipStream_t stream) {
  fused_block_kernel<<<256, 512, 0, stream>>>(d_in[0], d_in[1], d_in[2], d_in[3], d_in[4], d_in[6], d_in[7], d_in[8], d_in[9], d_out, d_ws);

}
```
